```python
import torch

def _softmax(x, axis=-1):
    return torch.softmax(x, dim=axis)
def _gelu(x):
    return torch.nn.functional.gelu(x, approximate="tanh")
def _silu(x):
    return torch.nn.functional.silu(x)
def _sigmoid(x):
    return torch.sigmoid(x)
def _layernorm(x, axis=-1, eps=1e-5):
    m = x.mean(dim=axis, keepdim=True)
    v = x.var(dim=axis, keepdim=True, unbiased=False)
    return (x - m) / torch.sqrt(v + eps)
def _scatter_add(tgt, idx, src, axis=0):
    return tgt.clone().index_add_(axis, idx, src)
def _segment_sum(src, seg, n):
    out = torch.zeros((n,) + src.shape[1:], dtype=src.dtype)
    return out.index_add_(0, seg, src)
def _pad(x, pw):
    flat = tuple(v for pair in reversed(pw) for v in pair)
    return torch.nn.functional.pad(x, flat)

kernel_name = 'standalone__mha_softmax__near_ridge'

def setup_inputs():

    _w = (lambda a: (lambda t: t.to(torch.bfloat16) if t.is_floating_point() else t)(torch.as_tensor(a)))
    B, H, Sq, Sk, D = 8, 32, 368, 512, 128
    return {
        "q": _w((lambda *s: torch.randn(*s, dtype=torch.bfloat16) * 0.8)(B, H, Sq, D)),
        "k": _w((lambda *s: torch.randn(*s, dtype=torch.bfloat16) * 0.8)(B, H, Sk, D)),
        "v": _w((lambda *s: torch.randn(*s, dtype=torch.bfloat16) * 0.05)(B, H, Sk, D)),
    }

def reference(q, k, v):
    scale = q.shape[-1] ** -0.5
    scores = torch.einsum("bhqd,bhkd->bhqk", q, k) * scale
    p = _softmax(scores, axis=-1)
    return torch.einsum("bhqk,bhkd->bhqd", p, v)
```

```cpp
#include <hip/hip_runtime.h>
#include <hip/hip_bf16.h>
#include <cstdio>
using bf16 = __hip_bfloat16;
constexpr int   D = 128, NW = 8, QBLK = 32, KVBLK = 64;
constexpr float SCALE = 0.088388347648318440f;
constexpr float THR = 8.f;
constexpr int SDEPTH = 2;
constexpr int B = 8, HQ = 32, S = 368, SKV = 512;
constexpr int LDQ = D, LDK = D, LDO = D;
constexpr size_t SHM_V = KVBLK * D * 2, SHM_K = KVBLK * D * 2, SHM_ATTN = 2 * SHM_V + 2 * SHM_K + NW * 64 * 4;
using bf16x8 = __attribute__((ext_vector_type(8))) short;
using s16x4  = __attribute__((ext_vector_type(4))) short;
using f32x16 = __attribute__((ext_vector_type(16))) float;
using f32x8  = __attribute__((ext_vector_type(8))) float;
using u32x4  = __attribute__((ext_vector_type(4))) unsigned;
#define KSWZ(row, colB) ((row) * 256 + ((colB) ^ (((row) & 7) << 4)))
#define SBAR() __builtin_amdgcn_sched_barrier(0)
__device__ __forceinline__ int crow(int r, int hi) { return (r & 3) + 8 * (r >> 2) + 4 * hi; }
__device__ __forceinline__ unsigned cvtpk(float lo, float hi) {
  unsigned r; asm volatile("v_cvt_pk_bf16_f32 %0, %1, %2" : "=v"(r) : "v"(lo), "v"(hi)); return r;
}
__device__ __forceinline__ bf16x8 ld8(const bf16* p) { return *reinterpret_cast<const bf16x8*>(p); }
__device__ __forceinline__ void partialSM(f32x16& p0, f32x16& p1, float& m_reg, float& mn, float& alpha) {
  constexpr float C = SCALE * 1.4426950408889634f;
  float pmax = p0[0]; for (int r = 1; r < 16; ++r) pmax = fmaxf(pmax, p0[r]); for (int r = 0; r < 16; ++r) pmax = fmaxf(pmax, p1[r]);
  { auto rr = __builtin_amdgcn_permlane32_swap(__float_as_uint(pmax), __float_as_uint(pmax), false, false);
    pmax = fmaxf(__uint_as_float(rr[0]), __uint_as_float(rr[1])); }
  if (__builtin_expect(__all(pmax - m_reg <= THR / SCALE), 1)) { mn = m_reg; alpha = 1.f; }
  else { mn = fmaxf(m_reg, pmax); alpha = __builtin_amdgcn_exp2f((m_reg - mn) * C); m_reg = mn; }
  float mnC = -mn * C;
  for (int r = 0; r < 16; ++r) p0[r] = fmaf(p0[r], C, mnC); for (int r = 0; r < 16; ++r) p1[r] = fmaf(p1[r], C, mnC);
  for (int r = 0; r < 16; ++r) p0[r] = __builtin_amdgcn_exp2f(p0[r]);
}
__device__ __forceinline__ void finishSM(f32x16& p0, f32x16& p1, float alpha, float& l_reg, bf16x8& pa0, bf16x8& pa1, bf16x8& pa2, bf16x8& pa3) {
  for (int r = 0; r < 16; ++r) p1[r] = __builtin_amdgcn_exp2f(p1[r]);
  float ps = 0; for (int r = 0; r < 16; ++r) ps += p0[r]; for (int r = 0; r < 16; ++r) ps += p1[r];
  { auto rr = __builtin_amdgcn_permlane32_swap(__float_as_uint(ps), __float_as_uint(ps), false, false);
    ps = __uint_as_float(rr[0]) + __uint_as_float(rr[1]); }
  l_reg = l_reg * alpha + ps;
#define PK4(P, BASE, OUT) do { unsigned a0 = cvtpk(P[BASE + 0], P[BASE + 1]), a1 = cvtpk(P[BASE + 2], P[BASE + 3]);   \
    unsigned b0 = cvtpk(P[BASE + 4], P[BASE + 5]), b1 = cvtpk(P[BASE + 6], P[BASE + 7]);                              \
    auto r0 = __builtin_amdgcn_permlane32_swap(a0, b0, false, false); auto r1 = __builtin_amdgcn_permlane32_swap(a1, b1, false, false); \
    u32x4 w = {r0[0], r1[0], r0[1], r1[1]}; OUT = *reinterpret_cast<bf16x8*>(&w); } while (0)
  PK4(p0, 0, pa0); PK4(p0, 8, pa1); PK4(p1, 0, pa2); PK4(p1, 8, pa3);
#undef PK4
}
__device__ __forceinline__ void qkt(f32x16& p0, f32x16& p1, const bf16* Ks, const bf16x8* qr, int r32, int hi) {
  p0 = f32x16{}; p1 = f32x16{};
  for (int d0 = 0; d0 < 8; ++d0) { int cb = (d0 * 16 + hi * 8) * 2;
    bf16x8 b0 = *reinterpret_cast<const bf16x8*>((const char*)Ks + KSWZ(r32, cb));
    bf16x8 b1 = *reinterpret_cast<const bf16x8*>((const char*)Ks + KSWZ(32 + r32, cb));
    p0 = __builtin_amdgcn_mfma_f32_32x32x16_bf16(b0, qr[d0], p0, 0, 0, 0);
    p1 = __builtin_amdgcn_mfma_f32_32x32x16_bf16(b1, qr[d0], p1, 0, 0, 0); }
}
__device__ __forceinline__ int v_st(int k, int c) { const int kk = (k & ~0xC) | ((k & 4) << 1) | ((k & 8) >> 1); return ((kk >> 3) * 4 + (c >> 5)) * 512 + ((kk & 7) * 32 + (c & 31)) * 2; }
__device__ __forceinline__ int v_rd_base(int lane) { return ((lane & 3) << 3) | (((lane >> 2) & 3) << 6) | (((lane >> 4) & 1) << 5) | (((lane >> 5) & 1) << 8); }
constexpr int v_rd_off(int d0, int ks, int half) { return d0 * 512 + ks * 4096 + half * 2048; }
template <int OFF> __device__ __forceinline__ s16x4 tr_read(int vb) {
  s16x4 r; asm volatile("ds_read_b64_tr_b16 %0, %1 offset:%2" : "=&v"(r) : "v"(vb), "i"(OFF) : "memory"); return r;
}
template <int D0> __device__ __forceinline__ void pv_one(f32x16& od, int vb, bf16x8 pa0, bf16x8 pa1, bf16x8 pa2, bf16x8 pa3) {
  const s16x4 l0 = tr_read<v_rd_off(D0, 0, 0)>(vb), h0 = tr_read<v_rd_off(D0, 0, 1)>(vb), l1 = tr_read<v_rd_off(D0, 1, 0)>(vb), h1 = tr_read<v_rd_off(D0, 1, 1)>(vb);
  const s16x4 l2 = tr_read<v_rd_off(D0, 2, 0)>(vb), h2 = tr_read<v_rd_off(D0, 2, 1)>(vb), l3 = tr_read<v_rd_off(D0, 3, 0)>(vb), h3 = tr_read<v_rd_off(D0, 3, 1)>(vb);
  asm volatile("s_waitcnt lgkmcnt(0)" ::: "memory"); SBAR();
#define PK(L, H) (bf16x8){L[0], L[1], L[2], L[3], H[0], H[1], H[2], H[3]}
  od = __builtin_amdgcn_mfma_f32_32x32x16_bf16(pa0, PK(l0, h0), od, 0, 0, 0);
  od = __builtin_amdgcn_mfma_f32_32x32x16_bf16(pa1, PK(l1, h1), od, 0, 0, 0);
  od = __builtin_amdgcn_mfma_f32_32x32x16_bf16(pa2, PK(l2, h2), od, 0, 0, 0);
  od = __builtin_amdgcn_mfma_f32_32x32x16_bf16(pa3, PK(l3, h3), od, 0, 0, 0);
#undef PK
}
__device__ __forceinline__ void pv_d0(f32x16* o, int vb, bf16x8 pa0, bf16x8 pa1, bf16x8 pa2, bf16x8 pa3) {
  pv_one<0>(o[0], vb, pa0, pa1, pa2, pa3); pv_one<1>(o[1], vb, pa0, pa1, pa2, pa3); pv_one<2>(o[2], vb, pa0, pa1, pa2, pa3); pv_one<3>(o[3], vb, pa0, pa1, pa2, pa3);
}
__device__ __forceinline__ void attn_dense_body(const bf16* __restrict__ Qb, const bf16* __restrict__ Kh, const bf16* __restrict__ Vh,
                                                bf16* __restrict__ Ob, int nrows, int seq, char* lds) {
  const int tid = threadIdx.x, wid = tid >> 6, lane = tid & 63, r32 = lane & 31, hi = lane >> 5;
  bf16* V_lds = (bf16*)lds; bf16* K_lds = (bf16*)(lds + 2 * SHM_V);
  float* ws = (float*)(lds + 2 * SHM_V + 2 * SHM_K) + wid * 64; float* li_l = ws; float* al_l = ws + 32;
  float m_reg = -1e30f, l_reg = 0; f32x16 o[4] = {}; bf16x8 qr[8];
  int qrow = wid * QBLK + r32; if (qrow >= nrows) qrow = nrows - 1;
  const bf16* Qw = Qb + (long)qrow * LDQ + hi * 8;
#pragma unroll
  for (int d0 = 0; d0 < 8; ++d0) qr[d0] = ld8(Qw + d0 * 16);
  const int sr = tid >> 4, sc = (tid & 15) * 8, vst0 = v_st(sr, sc), vst1 = v_st(32 + sr, sc);
  const int vb0 = (int)(uintptr_t)V_lds + v_rd_base(lane);
  struct { bf16x8 vs0, vs1, ks0, ks1; } sr_[SDEPTH];
#define SLOAD(i, k0) do { sr_[i].vs0 = ld8(&Vh[(long)((k0) + sr) * LDK + sc]); sr_[i].vs1 = ld8(&Vh[(long)((k0) + 32 + sr) * LDK + sc]); \
    sr_[i].ks0 = ld8(&Kh[(long)((k0) + sr) * LDK + sc]); sr_[i].ks1 = ld8(&Kh[(long)((k0) + 32 + sr) * LDK + sc]); } while (0)
#define SWRITE(b, i) do { *(bf16x8*)((char*)V_lds + (b) * SHM_V + vst0) = (sr_[i].vs0);          \
    *(bf16x8*)((char*)V_lds + (b) * SHM_V + vst1) = (sr_[i].vs1); int kc = sc * 2;               \
    *(bf16x8*)((char*)K_lds + (b) * SHM_K + KSWZ(sr, kc)) = (sr_[i].ks0);                       \
    *(bf16x8*)((char*)K_lds + (b) * SHM_K + KSWZ(32 + sr, kc)) = (sr_[i].ks1); } while (0)
#define SWAIT() do { asm volatile("s_waitcnt vmcnt(4)" ::: "memory"); } while (0)
#define RESC(a) do { if (__any((a) < 1.f)) { if (hi == 0) al_l[r32] = (a); asm volatile("s_waitcnt lgkmcnt(0)" ::: "memory"); \
    for (int d = 0; d < 4; ++d) for (int r = 0; r < 16; ++r) o[d][r] *= al_l[crow(r, hi)]; } } while (0)
  f32x16 pA0, pA1, pB0, pB1; float mnA, mnB, alA, alB; bf16x8 pa0, pa1, pa2, pa3; const int NT = seq / KVBLK;
  constexpr int SE = 0, SO = SDEPTH - 1;
  SLOAD(SE, 0); asm volatile("s_waitcnt vmcnt(0)" ::: "memory"); SWRITE(0, SE); __syncthreads();
  qkt(pA0, pA1, K_lds, qr, r32, hi); partialSM(pA0, pA1, m_reg, mnA, alA);
  SLOAD(SO, KVBLK); if (2 < NT) SLOAD(SE, 2 * KVBLK);
  SWAIT(); SWRITE(1, SO); __syncthreads();
  for (int j = 1; j + 1 < NT; j += 2) {
    SBAR(); qkt(pB0, pB1, (bf16*)((char*)K_lds + SHM_K), qr, r32, hi);
    finishSM(pA0, pA1, alA, l_reg, pa0, pa1, pa2, pa3); SBAR();
    SLOAD(SO, (j + SDEPTH) * KVBLK); SBAR();
    pv_d0(o, vb0, pa0, pa1, pa2, pa3); partialSM(pB0, pB1, m_reg, mnB, alB);
    __syncthreads(); SWAIT(); SWRITE(0, SE);
    RESC(alB); __syncthreads();
    SBAR(); qkt(pA0, pA1, K_lds, qr, r32, hi);
    finishSM(pB0, pB1, alB, l_reg, pa0, pa1, pa2, pa3); SBAR();
    if (j + 3 < NT) SLOAD(SE, (j + 1 + SDEPTH) * KVBLK); SBAR();
    pv_d0(o, vb0 + (int)SHM_V, pa0, pa1, pa2, pa3); partialSM(pA0, pA1, m_reg, mnA, alA);
    __syncthreads(); SWAIT(); SWRITE(1, SO);
    RESC(alA); __syncthreads();
  }
  SBAR(); qkt(pB0, pB1, (bf16*)((char*)K_lds + SHM_K), qr, r32, hi);
  finishSM(pA0, pA1, alA, l_reg, pa0, pa1, pa2, pa3); SBAR();
  pv_d0(o, vb0, pa0, pa1, pa2, pa3); partialSM(pB0, pB1, m_reg, mnB, alB);
  __syncthreads(); RESC(alB);
  finishSM(pB0, pB1, alB, l_reg, pa0, pa1, pa2, pa3); SBAR();
  pv_d0(o, vb0 + (int)SHM_V, pa0, pa1, pa2, pa3);
  if (hi == 0) li_l[r32] = l_reg; asm volatile("s_waitcnt lgkmcnt(0)" ::: "memory");
  float rli[16];
#pragma unroll
  for (int r = 0; r < 16; ++r) rli[r] = __builtin_amdgcn_rcpf(li_l[crow(r, hi)]);
  bf16* Ow = Ob + (long)(wid * QBLK) * LDO;
#pragma unroll
  for (int r = 0; r < 16; ++r) { int orow = crow(r, hi);
    if (wid * QBLK + orow < nrows)
    for (int d0 = 0; d0 < 4; ++d0) Ow[(long)orow * LDO + d0 * 32 + r32] = __float2bfloat16(o[d0][r] * rli[r]); }
}
__global__ __launch_bounds__(64 * NW, 1)
void attn_fwd_dense(const bf16* __restrict__ Q, const bf16* __restrict__ K, const bf16* __restrict__ V, bf16* __restrict__ O) {
  extern __shared__ __attribute__((aligned(16))) char shm[];
  const long bh = blockIdx.y, i0 = (long)blockIdx.x * (QBLK * NW);
  const long q0 = (bh * S + i0) * D, k0 = bh * SKV * (long)D;
  int nrows = S - i0; if (nrows > 256) nrows = 256;
  attn_dense_body(Q + q0, K + k0, V + k0, O + q0, nrows, SKV, shm);
}
extern "C" void kernel_launch(void* const* d_in, const int* in_sizes, int n_in, void* d_out, int out_size, void* d_ws, size_t ws_size, hipStream_t stream) {
  hipFuncSetAttribute((const void*)attn_fwd_dense, hipFuncAttributeMaxDynamicSharedMemorySize, (int)SHM_ATTN);
  hipLaunchKernelGGL(attn_fwd_dense, dim3(2, B * HQ), dim3(64 * NW), SHM_ATTN, stream,
                     (const bf16*)d_in[0], (const bf16*)d_in[1], (const bf16*)d_in[2], (bf16*)d_out);
}
```

```cpp
#include <hip/hip_runtime.h>
#include <hip/hip_bf16.h>
typedef unsigned short u16;
typedef float f32x4 __attribute__((ext_vector_type(4)));
typedef __bf16 bf16x8 __attribute__((ext_vector_type(8)));
typedef __bf16 bf16x2 __attribute__((ext_vector_type(2)));
typedef float f32x2 __attribute__((ext_vector_type(2)));
typedef unsigned u32x4 __attribute__((ext_vector_type(4)));
typedef unsigned u32x2 __attribute__((ext_vector_type(2)));
constexpr int SQ = 368, SK = 512, HD = 128, NBH = 256;
constexpr int LDS_BYTES = 163840;

__device__ __forceinline__ unsigned pack2(float lo, float hi) {
  f32x2 v = {lo, hi}; bf16x2 b = __builtin_convertvector(v, bf16x2); return __builtin_bit_cast(unsigned, b);
}
__device__ __forceinline__ void safe_rows_impl(const u16* __restrict__ Q, const u16* __restrict__ K, const u16* __restrict__ V, u16* __restrict__ O, int row0, int nqb) {
  const int lane = __builtin_amdgcn_mbcnt_hi(~0u, __builtin_amdgcn_mbcnt_lo(~0u, 0u));
  const int r = lane & 15, g = lane >> 4;
  const float C = 0.12751743082f;
#pragma unroll 1
  for (int jb = 0; jb < nqb; ++jb) {
    const int row = row0 + 16 * jb + r;
    bf16x8 qf[4];
#pragma unroll
    for (int ds = 0; ds < 4; ++ds) qf[ds] = *(const bf16x8*)(Q + row * HD + 32 * ds + 8 * g);
    float m = -__builtin_inff(), l = 0.f;
    f32x4 acc[8];
#pragma unroll
    for (int db = 0; db < 8; ++db) acc[db] = f32x4{0.f, 0.f, 0.f, 0.f};
#pragma unroll 1
    for (int t = 0; t < SK / 32; ++t) {
      f32x4 s0 = {0.f, 0.f, 0.f, 0.f}, s1 = {0.f, 0.f, 0.f, 0.f};
#pragma unroll
      for (int ds = 0; ds < 4; ++ds) {
        const bf16x8 k0 = *(const bf16x8*)(K + (32 * t + r) * HD + 32 * ds + 8 * g);
        const bf16x8 k1 = *(const bf16x8*)(K + (32 * t + 16 + r) * HD + 32 * ds + 8 * g);
        s0 = __builtin_amdgcn_mfma_f32_16x16x32_bf16(k0, qf[ds], s0, 0, 0, 0);
        s1 = __builtin_amdgcn_mfma_f32_16x16x32_bf16(k1, qf[ds], s1, 0, 0, 0);
      }
      float x[8];
#pragma unroll
      for (int i = 0; i < 4; ++i) { x[i] = s0[i] * C; x[4 + i] = s1[i] * C; }
      float tm = x[0];
#pragma unroll
      for (int i = 1; i < 8; ++i) tm = fmaxf(tm, x[i]);
      tm = fmaxf(tm, __shfl_xor(tm, 16)); tm = fmaxf(tm, __shfl_xor(tm, 32));
      const float mn = fmaxf(m, tm);
      const float alpha = __builtin_amdgcn_exp2f(m - mn);
      m = mn;
      float ps = 0.f;
#pragma unroll
      for (int i = 0; i < 8; ++i) { x[i] = __builtin_amdgcn_exp2f(x[i] - mn); ps += x[i]; }
      l = l * alpha + ps;
      const u32x4 pw = {pack2(x[0], x[1]), pack2(x[2], x[3]), pack2(x[4], x[5]), pack2(x[6], x[7])};
      const bf16x8 pf = __builtin_bit_cast(bf16x8, pw);
#pragma unroll
      for (int db = 0; db < 8; ++db) {
        u32x4 vw;
        const u16* vp = V + (32 * t + 4 * g) * HD + 16 * db + r;
        vw[0] = (unsigned)vp[0] | ((unsigned)vp[HD] << 16);
        vw[1] = (unsigned)vp[2 * HD] | ((unsigned)vp[3 * HD] << 16);
        vw[2] = (unsigned)vp[16 * HD] | ((unsigned)vp[17 * HD] << 16);
        vw[3] = (unsigned)vp[18 * HD] | ((unsigned)vp[19 * HD] << 16);
        acc[db] = acc[db] * alpha;
        acc[db] = __builtin_amdgcn_mfma_f32_16x16x32_bf16(__builtin_bit_cast(bf16x8, vw), pf, acc[db], 0, 0, 0);
      }
    }
    l += __shfl_xor(l, 16); l += __shfl_xor(l, 32);
    const float rl = 1.0f / l;
#pragma unroll
    for (int db = 0; db < 8; ++db) {
      const u32x2 w = {pack2(acc[db][0] * rl, acc[db][1] * rl), pack2(acc[db][2] * rl, acc[db][3] * rl)};
      *(u32x2*)(O + row * HD + 16 * db + 4 * g) = w;
    }
  }
}

__global__ __launch_bounds__(256, 1) void attn_fwd_kernel(const u16* __restrict__ q, const u16* __restrict__ k, const u16* __restrict__ v, u16* __restrict__ o) {
  extern __shared__ __attribute__((aligned(128))) char lds[];
  asm volatile("s_getpc_b64 s[52:53]\n .Lattn_pf_base:\n v_lshlrev_b32 v245, 7, %0\n v_min_u32 v245, .Lattn_main_end-.Lattn_pf_base-4, v245\n global_load_dword v246, v245, s[52:53]"
               :: "v"(threadIdx.x) : "s52", "s53", "v245", "v246", "memory");
  __builtin_amdgcn_sched_barrier(0);
  const int b = blockIdx.x;
  const int bh = (b & 7) * (NBH / 8) + (b >> 3);
  const int w = __builtin_amdgcn_readfirstlane(threadIdx.x >> 6);
  const u16* qh = q + (size_t)bh * SQ * HD;
  const u16* kh = k + (size_t)bh * SK * HD;
  const u16* vh = v + (size_t)bh * SK * HD;
  u16* oh = o + (size_t)bh * SQ * HD;
  const unsigned rot = bh & 15;
  (void)lds;
  unsigned unsafe;

  asm volatile(
    "v_mbcnt_lo_u32_b32 v255, -1, 0\n"
    "v_mbcnt_hi_u32_b32 v255, -1, v255\n"
    "s_mov_b64 s[20:21], %2\n"
    "s_mov_b64 s[24:25], %3\n"
    "s_mov_b64 s[28:29], %1\n"
    "s_mov_b64 s[96:97], %4\n"
    "s_mov_b32 s59, %5\n"
    "s_mov_b32 s58, 0\n"
    "s_lshl_b32 s94, %6, 13\n"
    "s_sub_u32 s20, s20, 2048\n"
    "s_subb_u32 s21, s21, 0\n"
    "s_and_b32 s21, s21, 0xffff\n"
    "s_mov_b32 s22, 133120\n"
    "s_mov_b32 s23, 0x20000\n"
    "s_sub_u32 s24, s24, 2048\n"
    "s_subb_u32 s25, s25, 0\n"
    "s_and_b32 s25, s25, 0xffff\n"
    "s_mov_b32 s26, 133120\n"
    "s_mov_b32 s27, 0x20000\n"
    "s_sub_u32 s28, s28, 2048\n"
    "s_subb_u32 s29, s29, 0\n"
    "s_and_b32 s29, s29, 0xffff\n"
    "s_mov_b32 s30, 96256\n"
    "s_mov_b32 s31, 0x20000\n"
    "s_lshl_b32 s40, s59, 12\n"
    "s_add_u32 s40, s40, s58\n"
    "s_and_b32 s41, s59, 1\n"
    "s_lshl_b32 s41, s41, 12\n"
    "s_mul_i32 s42, s59, 24576\n"
    "v_lshrrev_b32 v243, 4, v255\n"
    "v_and_b32 v242, 7, v255\n"
    "v_xor_b32 v242, v242, v243\n"
    "v_lshlrev_b32 v242, 4, v242\n"
    "v_lshrrev_b32 v244, 3, v255\n"
    "v_lshl_add_u32 v242, v244, 8, v242\n"
    "v_xor_b32 v244, 64, v242\n"
    "v_add_u32 v225, 2048, v242\n"
    "v_add_u32 v226, 3072, v244\n"
    "v_add_u32 v227, 128, v242\n"
    "v_add_u32 v228, 1152, v244\n"
    "v_bfe_u32 v242, v255, 1, 3\n"
    "v_lshlrev_b32 v242, 8, v242\n"
    "v_lshl_add_u32 v242, v243, 5, v242\n"
    "v_and_b32 v244, 1, v255\n"
    "v_lshl_add_u32 v242, v244, 4, v242\n"
    "v_add_u32 v229, 2048, v242\n"
    "v_add_u32 v230, 1152, v242\n"
    "s_cmp_ge_u32 s59, 2\n"
    "s_cselect_b32 s36, s24, s20\n"
    "s_cselect_b32 s37, s25, s21\n"
    "s_mov_b32 s38, 133120\n"
    "s_mov_b32 s39, 0x20000\n"
    "s_cselect_b64 vcc, -1, 0\n"
    "s_nop 1\n"
    "v_cndmask_b32 v231, v225, v229, vcc\n"
    "v_cndmask_b32 v232, v226, v230, vcc\n"
    "v_cndmask_b32 v233, v227, v229, vcc\n"
    "v_cndmask_b32 v234, v228, v230, vcc\n"
    "s_add_u32 s52, s94, 0\n"
    "s_and_b32 s52, s52, 131071\n"
    "s_add_u32 m0, s40, 32768\n"
    "s_add_u32 s43, s41, s52\n"
    "buffer_load_dwordx4 v231, s[36:39], s43 offen nt lds\n"
    "buffer_load_dwordx4 v232, s[36:39], s43 offen offset:1024 nt lds\n"
    "buffer_load_dwordx4 v233, s[36:39], s43 offen offset:2048 nt lds\n"
    "buffer_load_dwordx4 v234, s[36:39], s43 offen offset:3072 nt lds\n"
    "s_add_u32 s52, s94, 8192\n"
    "s_and_b32 s52, s52, 131071\n"
    "s_add_u32 m0, s40, 49152\n"
    "s_add_u32 s43, s41, s52\n"
    "buffer_load_dwordx4 v231, s[36:39], s43 offen nt lds\n"
    "buffer_load_dwordx4 v232, s[36:39], s43 offen offset:1024 nt lds\n"
    "buffer_load_dwordx4 v233, s[36:39], s43 offen offset:2048 nt lds\n"
    "buffer_load_dwordx4 v234, s[36:39], s43 offen offset:3072 nt lds\n"
    "s_add_u32 m0, s40, 65536\n"
    "s_add_u32 s43, s42, 0\n"
    "buffer_load_dwordx4 v225, s[28:31], s43 offen nt lds\n"
    "buffer_load_dwordx4 v226, s[28:31], s43 offen offset:1024 nt lds\n"
    "buffer_load_dwordx4 v227, s[28:31], s43 offen offset:2048 nt lds\n"
    "buffer_load_dwordx4 v228, s[28:31], s43 offen offset:3072 nt lds\n"
    "s_and_b32 s97, s97, 0xffff\n"
    "s_mov_b32 s98, 94208\n"
    "s_mov_b32 s99, 0x20000\n"
    "s_mov_b32 s50, 0x3e0293ee\n"
    "s_mov_b32 s51, 0x3e0293ee\n"
    "s_mov_b32 s95, 0xffff0000\n"
    "v_and_b32 v242, 15, v255\n"
    "v_lshrrev_b32 v243, 4, v255\n"
    "v_lshrrev_b32 v244, 1, v242\n"
    "v_xor_b32 v244, v243, v244\n"
    "v_lshlrev_b32 v244, 4, v244\n"
    "v_lshl_add_u32 v237, v242, 7, v244\n"
    "v_add_u32 v237, s58, v237\n"
    "v_xor_b32 v238, 64, v237\n"
    "v_lshrrev_b32 v242, 5, v255\n"
    "v_lshlrev_b32 v239, 11, v242\n"
    "v_bfe_u32 v242, v255, 4, 1\n"
    "v_lshl_add_u32 v239, v242, 7, v239\n"
    "v_bfe_u32 v242, v255, 2, 2\n"
    "v_lshl_add_u32 v239, v242, 5, v239\n"
    "v_and_b32 v242, 3, v255\n"
    "v_lshl_add_u32 v239, v242, 3, v239\n"
    "v_add_u32 v239, s58, v239\n"
    "v_mov_b32 v222, v237\n"
    "v_mov_b32 v223, v238\n"
    "v_mov_b32 v224, v239\n"
    "v_subrev_u32 v242, s58, v237\n"
    "v_add_u32 v235, s40, v242\n"
    "v_xor_b32 v236, 64, v235\n"
    "s_cmp_ge_u32 s59, 2\n"
    "s_cselect_b32 s93, 32768, 0\n"
    "s_add_u32 s93, s93, s42\n"
    "s_add_u32 s93, s93, s58\n"
    "v_and_b32 v242, 15, v255\n"
    "v_mul_u32_u24 v247, 272, v242\n"
    "v_lshrrev_b32 v243, 4, v255\n"
    "v_lshl_add_u32 v247, v243, 4, v247\n"
    "v_add_u32 v247, s93, v247\n"
    "v_mul_u32_u24 v248, 272, v243\n"
    "v_bfe_u32 v242, v255, 2, 2\n"
    "v_lshl_add_u32 v248, v242, 6, v248\n"
    "v_and_b32 v242, 1, v255\n"
    "v_lshl_add_u32 v248, v242, 5, v248\n"
    "v_bfe_u32 v242, v255, 1, 1\n"
    "v_lshl_add_u32 v248, v242, 3, v248\n"
    "v_add_u32 v248, s93, v248\n"
    "s_waitcnt vmcnt(8)\n"
    "s_add_u32 m0, s40, 81920\n"
    "s_add_u32 s43, s42, 4096\n"
    "buffer_load_dwordx4 v225, s[28:31], s43 offen nt lds\n"
    "buffer_load_dwordx4 v226, s[28:31], s43 offen offset:1024 nt lds\n"
    "buffer_load_dwordx4 v227, s[28:31], s43 offen offset:2048 nt lds\n"
    "buffer_load_dwordx4 v228, s[28:31], s43 offen offset:3072 nt lds\n"
    "s_add_u32 m0, s40, 98304\n"
    "s_add_u32 s43, s42, 8192\n"
    "buffer_load_dwordx4 v225, s[28:31], s43 offen nt lds\n"
    "buffer_load_dwordx4 v226, s[28:31], s43 offen offset:1024 nt lds\n"
    "buffer_load_dwordx4 v227, s[28:31], s43 offen offset:2048 nt lds\n"
    "buffer_load_dwordx4 v228, s[28:31], s43 offen offset:3072 nt lds\n"
    "s_waitcnt vmcnt(12)\n"
    "s_barrier\n"
    "s_add_u32 m0, s40, 131072\n"
    "s_add_u32 s43, s42, 12288\n"
    "buffer_load_dwordx4 v225, s[28:31], s43 offen nt lds\n"
    "buffer_load_dwordx4 v226, s[28:31], s43 offen offset:1024 nt lds\n"
    "buffer_load_dwordx4 v227, s[28:31], s43 offen offset:2048 nt lds\n"
    "buffer_load_dwordx4 v228, s[28:31], s43 offen offset:3072 nt lds\n"
    "ds_read_b128 a[96:99], v222 offset:32768\n"
    "ds_read_b128 a[100:103], v223 offset:32768\n"
    "ds_read_b128 a[104:107], v222 offset:34816\n"
    "ds_read_b128 a[108:111], v223 offset:34816\n"
    "ds_read_b128 a[112:115], v222 offset:36864\n"
    "ds_read_b128 a[116:119], v223 offset:36864\n"
    "ds_read_b128 a[120:123], v222 offset:38912\n"
    "ds_read_b128 a[124:127], v223 offset:38912\n"
    "ds_read_b128 a[128:131], v222 offset:49152\n"
    "ds_read_b128 a[132:135], v223 offset:49152\n"
    "ds_read_b128 a[136:139], v222 offset:51200\n"
    "ds_read_b128 a[140:143], v223 offset:51200\n"
    "ds_read_b128 a[144:147], v222 offset:53248\n"
    "ds_read_b128 a[148:151], v223 offset:53248\n"
    "ds_read_b128 a[152:155], v222 offset:55296\n"
    "ds_read_b128 a[156:159], v223 offset:55296\n"
    "s_waitcnt vmcnt(12)\n"
    "v_add_u32 v242, 65536, v235\n"
    "v_add_u32 v243, 65536, v236\n"
    "ds_read_b128 v[0:3], v242 offset:0\n"
    "ds_read_b128 v[4:7], v243 offset:0\n"
    "ds_read_b128 v[8:11], v242 offset:2048\n"
    "ds_read_b128 v[12:15], v243 offset:2048\n"
    "s_add_u32 m0, s40, 147456\n"
    "s_add_u32 s43, s42, 16384\n"
    "buffer_load_dwordx4 v225, s[28:31], s43 offen nt lds\n"
    "buffer_load_dwordx4 v226, s[28:31], s43 offen offset:1024 nt lds\n"
    "buffer_load_dwordx4 v227, s[28:31], s43 offen offset:2048 nt lds\n"
    "buffer_load_dwordx4 v228, s[28:31], s43 offen offset:3072 nt lds\n"
    "DS0%=:\n"
    "s_waitcnt lgkmcnt(0)\n"
    "v_lshlrev_b32 v16, 16, v0\n"
    "v_lshlrev_b32 v18, 16, v1\n"
    "ds_read_b64_tr_b16 a[160:161], v224 offset:40960\n"
    "v_lshlrev_b32 v20, 16, v2\n"
    "v_lshlrev_b32 v22, 16, v3\n"
    "ds_read_b64_tr_b16 a[162:163], v224 offset:45056\n"
    "v_and_b32 v17, s95, v0\n"
    "v_and_b32 v19, s95, v1\n"
    "ds_read_b64_tr_b16 a[164:165], v224 offset:41216\n"
    "v_and_b32 v21, s95, v2\n"
    "v_and_b32 v23, s95, v3\n"
    "ds_read_b64_tr_b16 a[166:167], v224 offset:45312\n"
    "v_pk_mul_f32 v[16:17], v[16:17], s[50:51]\n"
    "v_pk_mul_f32 v[18:19], v[18:19], s[50:51]\n"
    "ds_read_b64_tr_b16 a[168:169], v224 offset:41472\n"
    "v_pk_mul_f32 v[20:21], v[20:21], s[50:51]\n"
    "v_pk_mul_f32 v[22:23], v[22:23], s[50:51]\n"
    "ds_read_b64_tr_b16 a[170:171], v224 offset:45568\n"
    "v_cvt_pk_bf16_f32 v24, v16, v17\n"
    "v_cvt_pk_bf16_f32 v25, v18, v19\n"
    "ds_read_b64_tr_b16 a[172:173], v224 offset:41728\n"
    "v_cvt_pk_bf16_f32 v26, v20, v21\n"
    "v_cvt_pk_bf16_f32 v27, v22, v23\n"
    "ds_read_b64_tr_b16 a[174:175], v224 offset:45824\n"
    "v_accvgpr_write_b32 a0, v24\n"
    "v_accvgpr_write_b32 a1, v25\n"
    "v_accvgpr_write_b32 a2, v26\n"
    "v_accvgpr_write_b32 a3, v27\n"
    "v_lshlrev_b32 v16, 16, v4\n"
    "v_lshlrev_b32 v18, 16, v5\n"
    "v_mfma_f32_16x16x32_bf16 v[192:195], a[96:99], a[0:3], 0\n"
    "ds_read_b64_tr_b16 a[176:177], v224 offset:41984\n"
    "v_lshlrev_b32 v20, 16, v6\n"
    "v_lshlrev_b32 v22, 16, v7\n"
    "ds_read_b64_tr_b16 a[178:179], v224 offset:46080\n"
    "v_and_b32 v17, s95, v4\n"
    "v_mfma_f32_16x16x32_bf16 v[196:199], a[112:115], a[0:3], 0\n"
    "v_and_b32 v19, s95, v5\n"
    "ds_read_b64_tr_b16 a[180:181], v224 offset:42240\n"
    "v_and_b32 v21, s95, v6\n"
    "v_and_b32 v23, s95, v7\n"
    "ds_read_b64_tr_b16 a[182:183], v224 offset:46336\n"
    "v_pk_mul_f32 v[16:17], v[16:17], s[50:51]\n"
    "v_pk_mul_f32 v[18:19], v[18:19], s[50:51]\n"
    "ds_read_b64_tr_b16 a[184:185], v224 offset:42496\n"
    "v_pk_mul_f32 v[20:21], v[20:21], s[50:51]\n"
    "v_pk_mul_f32 v[22:23], v[22:23], s[50:51]\n"
    "ds_read_b64_tr_b16 a[186:187], v224 offset:46592\n"
    "v_cvt_pk_bf16_f32 v24, v16, v17\n"
    "v_cvt_pk_bf16_f32 v25, v18, v19\n"
    "ds_read_b64_tr_b16 a[188:189], v224 offset:42752\n"
    "v_cvt_pk_bf16_f32 v26, v20, v21\n"
    "v_mfma_f32_16x16x32_bf16 v[200:203], a[128:131], a[0:3], 0\n"
    "v_cvt_pk_bf16_f32 v27, v22, v23\n"
    "ds_read_b64_tr_b16 a[190:191], v224 offset:46848\n"
    "v_accvgpr_write_b32 a4, v24\n"
    "v_accvgpr_write_b32 a5, v25\n"
    "v_accvgpr_write_b32 a6, v26\n"
    "v_mfma_f32_16x16x32_bf16 v[204:207], a[144:147], a[0:3], 0\n"
    "v_accvgpr_write_b32 a7, v27\n"
    "v_lshlrev_b32 v16, 16, v8\n"
    "v_lshlrev_b32 v18, 16, v9\n"
    "v_mfma_f32_16x16x32_bf16 v[192:195], a[100:103], a[4:7], v[192:195]\n"
    "ds_read_b64_tr_b16 a[192:193], v224 offset:57344\n"
    "v_lshlrev_b32 v20, 16, v10\n"
    "v_lshlrev_b32 v22, 16, v11\n"
    "ds_read_b64_tr_b16 a[194:195], v224 offset:61440\n"
    "v_and_b32 v17, s95, v8\n"
    "v_mfma_f32_16x16x32_bf16 v[196:199], a[116:119], a[4:7], v[196:199]\n"
    "v_and_b32 v19, s95, v9\n"
    "ds_read_b64_tr_b16 a[196:197], v224 offset:57600\n"
    "v_and_b32 v21, s95, v10\n"
    "v_and_b32 v23, s95, v11\n"
    "ds_read_b64_tr_b16 a[198:199], v224 offset:61696\n"
    "v_pk_mul_f32 v[16:17], v[16:17], s[50:51]\n"
    "v_pk_mul_f32 v[18:19], v[18:19], s[50:51]\n"
    "ds_read_b64_tr_b16 a[200:201], v224 offset:57856\n"
    "v_pk_mul_f32 v[20:21], v[20:21], s[50:51]\n"
    "v_pk_mul_f32 v[22:23], v[22:23], s[50:51]\n"
    "ds_read_b64_tr_b16 a[202:203], v224 offset:61952\n"
    "v_cvt_pk_bf16_f32 v24, v16, v17\n"
    "v_cvt_pk_bf16_f32 v25, v18, v19\n"
    "ds_read_b64_tr_b16 a[204:205], v224 offset:58112\n"
    "v_cvt_pk_bf16_f32 v26, v20, v21\n"
    "v_mfma_f32_16x16x32_bf16 v[200:203], a[132:135], a[4:7], v[200:203]\n"
    "v_cvt_pk_bf16_f32 v27, v22, v23\n"
    "ds_read_b64_tr_b16 a[206:207], v224 offset:62208\n"
    "v_accvgpr_write_b32 a8, v24\n"
    "v_accvgpr_write_b32 a9, v25\n"
    "v_accvgpr_write_b32 a10, v26\n"
    "v_mfma_f32_16x16x32_bf16 v[204:207], a[148:151], a[4:7], v[204:207]\n"
    "v_accvgpr_write_b32 a11, v27\n"
    "v_lshlrev_b32 v16, 16, v12\n"
    "v_lshlrev_b32 v18, 16, v13\n"
    "v_mfma_f32_16x16x32_bf16 v[192:195], a[104:107], a[8:11], v[192:195]\n"
    "ds_read_b64_tr_b16 a[208:209], v224 offset:58368\n"
    "v_lshlrev_b32 v20, 16, v14\n"
    "v_lshlrev_b32 v22, 16, v15\n"
    "ds_read_b64_tr_b16 a[210:211], v224 offset:62464\n"
    "v_and_b32 v17, s95, v12\n"
    "v_mfma_f32_16x16x32_bf16 v[196:199], a[120:123], a[8:11], v[196:199]\n"
    "v_and_b32 v19, s95, v13\n"
    "ds_read_b64_tr_b16 a[212:213], v224 offset:58624\n"
    "v_and_b32 v21, s95, v14\n"
    "v_and_b32 v23, s95, v15\n"
    "ds_read_b64_tr_b16 a[214:215], v224 offset:62720\n"
    "v_pk_mul_f32 v[16:17], v[16:17], s[50:51]\n"
    "v_pk_mul_f32 v[18:19], v[18:19], s[50:51]\n"
    "ds_read_b64_tr_b16 a[216:217], v224 offset:58880\n"
    "v_pk_mul_f32 v[20:21], v[20:21], s[50:51]\n"
    "v_pk_mul_f32 v[22:23], v[22:23], s[50:51]\n"
    "ds_read_b64_tr_b16 a[218:219], v224 offset:62976\n"
    "v_cvt_pk_bf16_f32 v24, v16, v17\n"
    "v_cvt_pk_bf16_f32 v25, v18, v19\n"
    "ds_read_b64_tr_b16 a[220:221], v224 offset:59136\n"
    "v_cvt_pk_bf16_f32 v26, v20, v21\n"
    "v_mfma_f32_16x16x32_bf16 v[200:203], a[136:139], a[8:11], v[200:203]\n"
    "v_cvt_pk_bf16_f32 v27, v22, v23\n"
    "ds_read_b64_tr_b16 a[222:223], v224 offset:63232\n"
    "v_accvgpr_write_b32 a12, v24\n"
    "v_accvgpr_write_b32 a13, v25\n"
    "v_accvgpr_write_b32 a14, v26\n"
    "v_mfma_f32_16x16x32_bf16 v[204:207], a[152:155], a[8:11], v[204:207]\n"
    "v_accvgpr_write_b32 a15, v27\n"
    "s_nop 1\n"
    "v_mfma_f32_16x16x32_bf16 v[192:195], a[108:111], a[12:15], v[192:195]\n"
    "v_mfma_f32_16x16x32_bf16 v[196:199], a[124:127], a[12:15], v[196:199]\n"
    "v_mfma_f32_16x16x32_bf16 v[200:203], a[140:143], a[12:15], v[200:203]\n"
    "v_mfma_f32_16x16x32_bf16 v[204:207], a[156:159], a[12:15], v[204:207]\n"
    "s_nop 4\n"
    "v_exp_f32 v192, v192\n"
    "v_exp_f32 v193, v193\n"
    "v_exp_f32 v194, v194\n"
    "v_exp_f32 v195, v195\n"
    "v_cvt_pk_bf16_f32 v208, v192, v193\n"
    "v_mov_b32 v216, v192\n"
    "v_exp_f32 v196, v196\n"
    "v_cvt_pk_bf16_f32 v209, v194, v195\n"
    "v_add_f32 v216, v216, v193\n"
    "v_exp_f32 v197, v197\n"
    "v_add_f32 v216, v216, v194\n"
    "v_exp_f32 v198, v198\n"
    "v_add_f32 v216, v216, v195\n"
    "v_exp_f32 v199, v199\n"
    "v_add_f32 v216, v216, v196\n"
    "v_cvt_pk_bf16_f32 v210, v196, v197\n"
    "v_add_f32 v216, v216, v197\n"
    "v_add_f32 v216, v216, v198\n"
    "v_cvt_pk_bf16_f32 v211, v198, v199\n"
    "v_add_f32 v216, v216, v199\n"
    "s_waitcnt lgkmcnt(0)\n"
    "v_exp_f32 v200, v200\n"
    "v_exp_f32 v201, v201\n"
    "v_mfma_f32_16x16x32_bf16 v[0:3], a[160:163], v[208:211], 0\n"
    "v_exp_f32 v202, v202\n"
    "v_exp_f32 v203, v203\n"
    "v_mfma_f32_16x16x32_bf16 v[4:7], a[164:167], v[208:211], 0\n"
    "v_cvt_pk_bf16_f32 v212, v200, v201\n"
    "v_add_f32 v216, v216, v200\n"
    "v_mfma_f32_16x16x32_bf16 v[8:11], a[168:171], v[208:211], 0\n"
    "v_exp_f32 v204, v204\n"
    "v_cvt_pk_bf16_f32 v213, v202, v203\n"
    "v_mfma_f32_16x16x32_bf16 v[12:15], a[172:175], v[208:211], 0\n"
    "v_add_f32 v216, v216, v201\n"
    "v_exp_f32 v205, v205\n"
    "v_mfma_f32_16x16x32_bf16 v[16:19], a[176:179], v[208:211], 0\n"
    "v_add_f32 v216, v216, v202\n"
    "v_exp_f32 v206, v206\n"
    "v_mfma_f32_16x16x32_bf16 v[20:23], a[180:183], v[208:211], 0\n"
    "v_add_f32 v216, v216, v203\n"
    "v_exp_f32 v207, v207\n"
    "v_mfma_f32_16x16x32_bf16 v[24:27], a[184:187], v[208:211], 0\n"
    "v_add_f32 v216, v216, v204\n"
    "v_cvt_pk_bf16_f32 v214, v204, v205\n"
    "v_mfma_f32_16x16x32_bf16 v[28:31], a[188:191], v[208:211], 0\n"
    "v_add_f32 v216, v216, v205\n"
    "v_add_f32 v216, v216, v206\n"
    "v_cvt_pk_bf16_f32 v215, v206, v207\n"
    "v_add_f32 v216, v216, v207\n"
    "s_waitcnt vmcnt(12)\n"
    "v_add_u32 v242, 81920, v235\n"
    "v_add_u32 v243, 81920, v236\n"
    "ds_read_b128 v[32:35], v242 offset:0\n"
    "ds_read_b128 v[36:39], v243 offset:0\n"
    "ds_read_b128 v[40:43], v242 offset:2048\n"
    "ds_read_b128 v[44:47], v243 offset:2048\n"
    "s_cmp_eq_u32 s59, 3\n"
    "s_cbranch_scc1 OI1%=\n"
    "s_add_u32 m0, s40, 65536\n"
    "s_add_u32 s43, s42, 20480\n"
    "buffer_load_dwordx4 v225, s[28:31], s43 offen nt lds\n"
    "buffer_load_dwordx4 v226, s[28:31], s43 offen offset:1024 nt lds\n"
    "buffer_load_dwordx4 v227, s[28:31], s43 offen offset:2048 nt lds\n"
    "buffer_load_dwordx4 v228, s[28:31], s43 offen offset:3072 nt lds\n"
    "JI1%=:\n"
    "DS1%=:\n"
    "v_mfma_f32_16x16x32_bf16 v[0:3], a[192:195], v[212:215], v[0:3]\n"
    "v_mfma_f32_16x16x32_bf16 v[4:7], a[196:199], v[212:215], v[4:7]\n"
    "v_mfma_f32_16x16x32_bf16 v[8:11], a[200:203], v[212:215], v[8:11]\n"
    "v_mfma_f32_16x16x32_bf16 v[12:15], a[204:207], v[212:215], v[12:15]\n"
    "v_mfma_f32_16x16x32_bf16 v[16:19], a[208:211], v[212:215], v[16:19]\n"
    "v_mfma_f32_16x16x32_bf16 v[20:23], a[212:215], v[212:215], v[20:23]\n"
    "v_mfma_f32_16x16x32_bf16 v[24:27], a[216:219], v[212:215], v[24:27]\n"
    "v_mfma_f32_16x16x32_bf16 v[28:31], a[220:223], v[212:215], v[28:31]\n"
    "s_waitcnt lgkmcnt(3)\n"
    "v_lshlrev_b32 v48, 16, v32\n"
    "v_lshlrev_b32 v50, 16, v33\n"
    "v_lshlrev_b32 v52, 16, v34\n"
    "v_lshlrev_b32 v54, 16, v35\n"
    "v_and_b32 v49, s95, v32\n"
    "v_and_b32 v51, s95, v33\n"
    "v_and_b32 v53, s95, v34\n"
    "v_and_b32 v55, s95, v35\n"
    "v_pk_mul_f32 v[48:49], v[48:49], s[50:51]\n"
    "v_pk_mul_f32 v[50:51], v[50:51], s[50:51]\n"
    "v_pk_mul_f32 v[52:53], v[52:53], s[50:51]\n"
    "v_pk_mul_f32 v[54:55], v[54:55], s[50:51]\n"
    "v_cvt_pk_bf16_f32 v56, v48, v49\n"
    "v_cvt_pk_bf16_f32 v57, v50, v51\n"
    "v_cvt_pk_bf16_f32 v58, v52, v53\n"
    "v_cvt_pk_bf16_f32 v59, v54, v55\n"
    "v_accvgpr_write_b32 a16, v56\n"
    "v_accvgpr_write_b32 a17, v57\n"
    "v_accvgpr_write_b32 a18, v58\n"
    "v_accvgpr_write_b32 a19, v59\n"
    "s_waitcnt lgkmcnt(2)\n"
    "v_lshlrev_b32 v48, 16, v36\n"
    "v_lshlrev_b32 v50, 16, v37\n"
    "v_mfma_f32_16x16x32_bf16 v[192:195], a[96:99], a[16:19], 0\n"
    "v_lshlrev_b32 v52, 16, v38\n"
    "v_lshlrev_b32 v54, 16, v39\n"
    "v_and_b32 v49, s95, v36\n"
    "v_mfma_f32_16x16x32_bf16 v[196:199], a[112:115], a[16:19], 0\n"
    "v_and_b32 v51, s95, v37\n"
    "v_and_b32 v53, s95, v38\n"
    "v_and_b32 v55, s95, v39\n"
    "v_pk_mul_f32 v[48:49], v[48:49], s[50:51]\n"
    "v_pk_mul_f32 v[50:51], v[50:51], s[50:51]\n"
    "v_pk_mul_f32 v[52:53], v[52:53], s[50:51]\n"
    "v_pk_mul_f32 v[54:55], v[54:55], s[50:51]\n"
    "v_cvt_pk_bf16_f32 v56, v48, v49\n"
    "v_cvt_pk_bf16_f32 v57, v50, v51\n"
    "v_cvt_pk_bf16_f32 v58, v52, v53\n"
    "v_mfma_f32_16x16x32_bf16 v[200:203], a[128:131], a[16:19], 0\n"
    "v_cvt_pk_bf16_f32 v59, v54, v55\n"
    "v_accvgpr_write_b32 a20, v56\n"
    "v_accvgpr_write_b32 a21, v57\n"
    "v_accvgpr_write_b32 a22, v58\n"
    "v_mfma_f32_16x16x32_bf16 v[204:207], a[144:147], a[16:19], 0\n"
    "v_accvgpr_write_b32 a23, v59\n"
    "s_waitcnt lgkmcnt(1)\n"
    "v_lshlrev_b32 v48, 16, v40\n"
    "v_lshlrev_b32 v50, 16, v41\n"
    "v_mfma_f32_16x16x32_bf16 v[192:195], a[100:103], a[20:23], v[192:195]\n"
    "v_lshlrev_b32 v52, 16, v42\n"
    "v_lshlrev_b32 v54, 16, v43\n"
    "v_and_b32 v49, s95, v40\n"
    "v_mfma_f32_16x16x32_bf16 v[196:199], a[116:119], a[20:23], v[196:199]\n"
    "v_and_b32 v51, s95, v41\n"
    "v_and_b32 v53, s95, v42\n"
    "v_and_b32 v55, s95, v43\n"
    "v_pk_mul_f32 v[48:49], v[48:49], s[50:51]\n"
    "v_pk_mul_f32 v[50:51], v[50:51], s[50:51]\n"
    "v_pk_mul_f32 v[52:53], v[52:53], s[50:51]\n"
    "v_pk_mul_f32 v[54:55], v[54:55], s[50:51]\n"
    "v_cvt_pk_bf16_f32 v56, v48, v49\n"
    "v_cvt_pk_bf16_f32 v57, v50, v51\n"
    "v_cvt_pk_bf16_f32 v58, v52, v53\n"
    "v_mfma_f32_16x16x32_bf16 v[200:203], a[132:135], a[20:23], v[200:203]\n"
    "v_cvt_pk_bf16_f32 v59, v54, v55\n"
    "v_accvgpr_write_b32 a24, v56\n"
    "v_accvgpr_write_b32 a25, v57\n"
    "v_accvgpr_write_b32 a26, v58\n"
    "v_mfma_f32_16x16x32_bf16 v[204:207], a[148:151], a[20:23], v[204:207]\n"
    "v_accvgpr_write_b32 a27, v59\n"
    "s_waitcnt lgkmcnt(0)\n"
    "v_lshlrev_b32 v48, 16, v44\n"
    "v_lshlrev_b32 v50, 16, v45\n"
    "v_mfma_f32_16x16x32_bf16 v[192:195], a[104:107], a[24:27], v[192:195]\n"
    "v_lshlrev_b32 v52, 16, v46\n"
    "v_lshlrev_b32 v54, 16, v47\n"
    "v_and_b32 v49, s95, v44\n"
    "v_mfma_f32_16x16x32_bf16 v[196:199], a[120:123], a[24:27], v[196:199]\n"
    "v_and_b32 v51, s95, v45\n"
    "v_and_b32 v53, s95, v46\n"
    "v_and_b32 v55, s95, v47\n"
    "v_pk_mul_f32 v[48:49], v[48:49], s[50:51]\n"
    "v_pk_mul_f32 v[50:51], v[50:51], s[50:51]\n"
    "v_pk_mul_f32 v[52:53], v[52:53], s[50:51]\n"
    "v_pk_mul_f32 v[54:55], v[54:55], s[50:51]\n"
    "v_cvt_pk_bf16_f32 v56, v48, v49\n"
    "v_cvt_pk_bf16_f32 v57, v50, v51\n"
    "v_cvt_pk_bf16_f32 v58, v52, v53\n"
    "v_mfma_f32_16x16x32_bf16 v[200:203], a[136:139], a[24:27], v[200:203]\n"
    "v_cvt_pk_bf16_f32 v59, v54, v55\n"
    "v_accvgpr_write_b32 a28, v56\n"
    "v_accvgpr_write_b32 a29, v57\n"
    "v_accvgpr_write_b32 a30, v58\n"
    "v_mfma_f32_16x16x32_bf16 v[204:207], a[152:155], a[24:27], v[204:207]\n"
    "v_accvgpr_write_b32 a31, v59\n"
    "s_nop 1\n"
    "v_mfma_f32_16x16x32_bf16 v[192:195], a[108:111], a[28:31], v[192:195]\n"
    "v_mfma_f32_16x16x32_bf16 v[196:199], a[124:127], a[28:31], v[196:199]\n"
    "v_mfma_f32_16x16x32_bf16 v[200:203], a[140:143], a[28:31], v[200:203]\n"
    "v_mfma_f32_16x16x32_bf16 v[204:207], a[156:159], a[28:31], v[204:207]\n"
    "s_nop 4\n"
    "v_exp_f32 v192, v192\n"
    "v_exp_f32 v193, v193\n"
    "v_exp_f32 v194, v194\n"
    "v_exp_f32 v195, v195\n"
    "v_cvt_pk_bf16_f32 v208, v192, v193\n"
    "v_mov_b32 v217, v192\n"
    "v_exp_f32 v196, v196\n"
    "v_cvt_pk_bf16_f32 v209, v194, v195\n"
    "v_add_f32 v217, v217, v193\n"
    "v_exp_f32 v197, v197\n"
    "v_add_f32 v217, v217, v194\n"
    "v_exp_f32 v198, v198\n"
    "v_add_f32 v217, v217, v195\n"
    "v_exp_f32 v199, v199\n"
    "v_add_f32 v217, v217, v196\n"
    "v_cvt_pk_bf16_f32 v210, v196, v197\n"
    "v_add_f32 v217, v217, v197\n"
    "v_add_f32 v217, v217, v198\n"
    "v_cvt_pk_bf16_f32 v211, v198, v199\n"
    "v_add_f32 v217, v217, v199\n"
    "v_exp_f32 v200, v200\n"
    "v_exp_f32 v201, v201\n"
    "v_mfma_f32_16x16x32_bf16 v[32:35], a[160:163], v[208:211], 0\n"
    "v_exp_f32 v202, v202\n"
    "v_exp_f32 v203, v203\n"
    "v_mfma_f32_16x16x32_bf16 v[36:39], a[164:167], v[208:211], 0\n"
    "v_cvt_pk_bf16_f32 v212, v200, v201\n"
    "v_add_f32 v217, v217, v200\n"
    "v_mfma_f32_16x16x32_bf16 v[40:43], a[168:171], v[208:211], 0\n"
    "v_exp_f32 v204, v204\n"
    "v_cvt_pk_bf16_f32 v213, v202, v203\n"
    "v_mfma_f32_16x16x32_bf16 v[44:47], a[172:175], v[208:211], 0\n"
    "v_add_f32 v217, v217, v201\n"
    "v_exp_f32 v205, v205\n"
    "v_mfma_f32_16x16x32_bf16 v[48:51], a[176:179], v[208:211], 0\n"
    "v_add_f32 v217, v217, v202\n"
    "v_exp_f32 v206, v206\n"
    "v_mfma_f32_16x16x32_bf16 v[52:55], a[180:183], v[208:211], 0\n"
    "v_add_f32 v217, v217, v203\n"
    "v_exp_f32 v207, v207\n"
    "v_mfma_f32_16x16x32_bf16 v[56:59], a[184:187], v[208:211], 0\n"
    "v_add_f32 v217, v217, v204\n"
    "v_cvt_pk_bf16_f32 v214, v204, v205\n"
    "v_mfma_f32_16x16x32_bf16 v[60:63], a[188:191], v[208:211], 0\n"
    "v_add_f32 v217, v217, v205\n"
    "v_add_f32 v217, v217, v206\n"
    "v_cvt_pk_bf16_f32 v215, v206, v207\n"
    "v_add_f32 v217, v217, v207\n"
    "s_waitcnt vmcnt(12)\n"
    "v_add_u32 v242, 98304, v235\n"
    "v_add_u32 v243, 98304, v236\n"
    "ds_read_b128 v[64:67], v242 offset:0\n"
    "ds_read_b128 v[68:71], v243 offset:0\n"
    "ds_read_b128 v[72:75], v242 offset:2048\n"
    "ds_read_b128 v[76:79], v243 offset:2048\n"
    "s_cmp_eq_u32 s59, 3\n"
    "s_cbranch_scc1 OI2%=\n"
    "s_add_u32 s52, s94, 16384\n"
    "s_and_b32 s52, s52, 131071\n"
    "s_add_u32 m0, s40, 114688\n"
    "s_add_u32 s43, s41, s52\n"
    "buffer_load_dwordx4 v231, s[36:39], s43 offen nt lds\n"
    "buffer_load_dwordx4 v232, s[36:39], s43 offen offset:1024 nt lds\n"
    "buffer_load_dwordx4 v233, s[36:39], s43 offen offset:2048 nt lds\n"
    "buffer_load_dwordx4 v234, s[36:39], s43 offen offset:3072 nt lds\n"
    "JI2%=:\n"
    "DS2%=:\n"
    "v_mfma_f32_16x16x32_bf16 v[32:35], a[192:195], v[212:215], v[32:35]\n"
    "v_mfma_f32_16x16x32_bf16 v[36:39], a[196:199], v[212:215], v[36:39]\n"
    "v_mfma_f32_16x16x32_bf16 v[40:43], a[200:203], v[212:215], v[40:43]\n"
    "v_mfma_f32_16x16x32_bf16 v[44:47], a[204:207], v[212:215], v[44:47]\n"
    "v_mfma_f32_16x16x32_bf16 v[48:51], a[208:211], v[212:215], v[48:51]\n"
    "v_mfma_f32_16x16x32_bf16 v[52:55], a[212:215], v[212:215], v[52:55]\n"
    "v_mfma_f32_16x16x32_bf16 v[56:59], a[216:219], v[212:215], v[56:59]\n"
    "v_mfma_f32_16x16x32_bf16 v[60:63], a[220:223], v[212:215], v[60:63]\n"
    "s_waitcnt lgkmcnt(3)\n"
    "v_lshlrev_b32 v80, 16, v64\n"
    "v_lshlrev_b32 v82, 16, v65\n"
    "v_lshlrev_b32 v84, 16, v66\n"
    "v_lshlrev_b32 v86, 16, v67\n"
    "v_and_b32 v81, s95, v64\n"
    "v_and_b32 v83, s95, v65\n"
    "v_and_b32 v85, s95, v66\n"
    "v_and_b32 v87, s95, v67\n"
    "v_pk_mul_f32 v[80:81], v[80:81], s[50:51]\n"
    "v_pk_mul_f32 v[82:83], v[82:83], s[50:51]\n"
    "v_pk_mul_f32 v[84:85], v[84:85], s[50:51]\n"
    "v_pk_mul_f32 v[86:87], v[86:87], s[50:51]\n"
    "v_cvt_pk_bf16_f32 v88, v80, v81\n"
    "v_cvt_pk_bf16_f32 v89, v82, v83\n"
    "v_cvt_pk_bf16_f32 v90, v84, v85\n"
    "v_cvt_pk_bf16_f32 v91, v86, v87\n"
    "v_accvgpr_write_b32 a32, v88\n"
    "v_accvgpr_write_b32 a33, v89\n"
    "v_accvgpr_write_b32 a34, v90\n"
    "v_accvgpr_write_b32 a35, v91\n"
    "s_waitcnt lgkmcnt(2)\n"
    "v_lshlrev_b32 v80, 16, v68\n"
    "v_lshlrev_b32 v82, 16, v69\n"
    "v_mfma_f32_16x16x32_bf16 v[192:195], a[96:99], a[32:35], 0\n"
    "v_lshlrev_b32 v84, 16, v70\n"
    "v_lshlrev_b32 v86, 16, v71\n"
    "v_and_b32 v81, s95, v68\n"
    "v_mfma_f32_16x16x32_bf16 v[196:199], a[112:115], a[32:35], 0\n"
    "v_and_b32 v83, s95, v69\n"
    "v_and_b32 v85, s95, v70\n"
    "v_and_b32 v87, s95, v71\n"
    "v_pk_mul_f32 v[80:81], v[80:81], s[50:51]\n"
    "v_pk_mul_f32 v[82:83], v[82:83], s[50:51]\n"
    "v_pk_mul_f32 v[84:85], v[84:85], s[50:51]\n"
    "v_pk_mul_f32 v[86:87], v[86:87], s[50:51]\n"
    "v_cvt_pk_bf16_f32 v88, v80, v81\n"
    "v_cvt_pk_bf16_f32 v89, v82, v83\n"
    "v_cvt_pk_bf16_f32 v90, v84, v85\n"
    "v_mfma_f32_16x16x32_bf16 v[200:203], a[128:131], a[32:35], 0\n"
    "v_cvt_pk_bf16_f32 v91, v86, v87\n"
    "v_accvgpr_write_b32 a36, v88\n"
    "v_accvgpr_write_b32 a37, v89\n"
    "v_accvgpr_write_b32 a38, v90\n"
    "v_mfma_f32_16x16x32_bf16 v[204:207], a[144:147], a[32:35], 0\n"
    "v_accvgpr_write_b32 a39, v91\n"
    "s_waitcnt lgkmcnt(1)\n"
    "v_lshlrev_b32 v80, 16, v72\n"
    "v_lshlrev_b32 v82, 16, v73\n"
    "v_mfma_f32_16x16x32_bf16 v[192:195], a[100:103], a[36:39], v[192:195]\n"
    "v_lshlrev_b32 v84, 16, v74\n"
    "v_lshlrev_b32 v86, 16, v75\n"
    "v_and_b32 v81, s95, v72\n"
    "v_mfma_f32_16x16x32_bf16 v[196:199], a[116:119], a[36:39], v[196:199]\n"
    "v_and_b32 v83, s95, v73\n"
    "v_and_b32 v85, s95, v74\n"
    "v_and_b32 v87, s95, v75\n"
    "v_pk_mul_f32 v[80:81], v[80:81], s[50:51]\n"
    "v_pk_mul_f32 v[82:83], v[82:83], s[50:51]\n"
    "v_pk_mul_f32 v[84:85], v[84:85], s[50:51]\n"
    "v_pk_mul_f32 v[86:87], v[86:87], s[50:51]\n"
    "v_cvt_pk_bf16_f32 v88, v80, v81\n"
    "v_cvt_pk_bf16_f32 v89, v82, v83\n"
    "v_cvt_pk_bf16_f32 v90, v84, v85\n"
    "v_mfma_f32_16x16x32_bf16 v[200:203], a[132:135], a[36:39], v[200:203]\n"
    "v_cvt_pk_bf16_f32 v91, v86, v87\n"
    "v_accvgpr_write_b32 a40, v88\n"
    "v_accvgpr_write_b32 a41, v89\n"
    "v_accvgpr_write_b32 a42, v90\n"
    "v_mfma_f32_16x16x32_bf16 v[204:207], a[148:151], a[36:39], v[204:207]\n"
    "v_accvgpr_write_b32 a43, v91\n"
    "s_waitcnt lgkmcnt(0)\n"
    "v_lshlrev_b32 v80, 16, v76\n"
    "v_lshlrev_b32 v82, 16, v77\n"
    "v_mfma_f32_16x16x32_bf16 v[192:195], a[104:107], a[40:43], v[192:195]\n"
    "v_lshlrev_b32 v84, 16, v78\n"
    "v_lshlrev_b32 v86, 16, v79\n"
    "v_and_b32 v81, s95, v76\n"
    "v_mfma_f32_16x16x32_bf16 v[196:199], a[120:123], a[40:43], v[196:199]\n"
    "v_and_b32 v83, s95, v77\n"
    "v_and_b32 v85, s95, v78\n"
    "v_and_b32 v87, s95, v79\n"
    "v_pk_mul_f32 v[80:81], v[80:81], s[50:51]\n"
    "v_pk_mul_f32 v[82:83], v[82:83], s[50:51]\n"
    "v_pk_mul_f32 v[84:85], v[84:85], s[50:51]\n"
    "v_pk_mul_f32 v[86:87], v[86:87], s[50:51]\n"
    "v_cvt_pk_bf16_f32 v88, v80, v81\n"
    "v_cvt_pk_bf16_f32 v89, v82, v83\n"
    "v_cvt_pk_bf16_f32 v90, v84, v85\n"
    "v_mfma_f32_16x16x32_bf16 v[200:203], a[136:139], a[40:43], v[200:203]\n"
    "v_cvt_pk_bf16_f32 v91, v86, v87\n"
    "v_accvgpr_write_b32 a44, v88\n"
    "v_accvgpr_write_b32 a45, v89\n"
    "v_accvgpr_write_b32 a46, v90\n"
    "v_mfma_f32_16x16x32_bf16 v[204:207], a[152:155], a[40:43], v[204:207]\n"
    "v_accvgpr_write_b32 a47, v91\n"
    "s_nop 1\n"
    "v_mfma_f32_16x16x32_bf16 v[192:195], a[108:111], a[44:47], v[192:195]\n"
    "v_mfma_f32_16x16x32_bf16 v[196:199], a[124:127], a[44:47], v[196:199]\n"
    "v_mfma_f32_16x16x32_bf16 v[200:203], a[140:143], a[44:47], v[200:203]\n"
    "v_mfma_f32_16x16x32_bf16 v[204:207], a[156:159], a[44:47], v[204:207]\n"
    "s_nop 4\n"
    "v_exp_f32 v192, v192\n"
    "v_exp_f32 v193, v193\n"
    "v_exp_f32 v194, v194\n"
    "v_exp_f32 v195, v195\n"
    "v_cvt_pk_bf16_f32 v208, v192, v193\n"
    "v_mov_b32 v218, v192\n"
    "v_exp_f32 v196, v196\n"
    "v_cvt_pk_bf16_f32 v209, v194, v195\n"
    "v_add_f32 v218, v218, v193\n"
    "v_exp_f32 v197, v197\n"
    "v_add_f32 v218, v218, v194\n"
    "v_exp_f32 v198, v198\n"
    "v_add_f32 v218, v218, v195\n"
    "v_exp_f32 v199, v199\n"
    "v_add_f32 v218, v218, v196\n"
    "v_cvt_pk_bf16_f32 v210, v196, v197\n"
    "v_add_f32 v218, v218, v197\n"
    "v_add_f32 v218, v218, v198\n"
    "v_cvt_pk_bf16_f32 v211, v198, v199\n"
    "v_add_f32 v218, v218, v199\n"
    "v_exp_f32 v200, v200\n"
    "v_exp_f32 v201, v201\n"
    "v_mfma_f32_16x16x32_bf16 v[64:67], a[160:163], v[208:211], 0\n"
    "v_exp_f32 v202, v202\n"
    "v_exp_f32 v203, v203\n"
    "v_mfma_f32_16x16x32_bf16 v[68:71], a[164:167], v[208:211], 0\n"
    "v_cvt_pk_bf16_f32 v212, v200, v201\n"
    "v_add_f32 v218, v218, v200\n"
    "v_mfma_f32_16x16x32_bf16 v[72:75], a[168:171], v[208:211], 0\n"
    "v_exp_f32 v204, v204\n"
    "v_cvt_pk_bf16_f32 v213, v202, v203\n"
    "v_mfma_f32_16x16x32_bf16 v[76:79], a[172:175], v[208:211], 0\n"
    "v_add_f32 v218, v218, v201\n"
    "v_exp_f32 v205, v205\n"
    "v_mfma_f32_16x16x32_bf16 v[80:83], a[176:179], v[208:211], 0\n"
    "v_add_f32 v218, v218, v202\n"
    "v_exp_f32 v206, v206\n"
    "v_mfma_f32_16x16x32_bf16 v[84:87], a[180:183], v[208:211], 0\n"
    "v_add_f32 v218, v218, v203\n"
    "v_exp_f32 v207, v207\n"
    "v_mfma_f32_16x16x32_bf16 v[88:91], a[184:187], v[208:211], 0\n"
    "v_add_f32 v218, v218, v204\n"
    "v_cvt_pk_bf16_f32 v214, v204, v205\n"
    "v_mfma_f32_16x16x32_bf16 v[92:95], a[188:191], v[208:211], 0\n"
    "v_add_f32 v218, v218, v205\n"
    "v_add_f32 v218, v218, v206\n"
    "v_cvt_pk_bf16_f32 v215, v206, v207\n"
    "v_add_f32 v218, v218, v207\n"
    "s_waitcnt vmcnt(12)\n"
    "v_add_u32 v242, 131072, v235\n"
    "v_add_u32 v243, 131072, v236\n"
    "ds_read_b128 v[96:99], v242 offset:0\n"
    "ds_read_b128 v[100:103], v243 offset:0\n"
    "ds_read_b128 v[104:107], v242 offset:2048\n"
    "ds_read_b128 v[108:111], v243 offset:2048\n"
    "s_cmp_eq_u32 s59, 3\n"
    "s_cbranch_scc1 OI3%=\n"
    "JI3%=:\n"
    "DS3%=:\n"
    "v_mfma_f32_16x16x32_bf16 v[64:67], a[192:195], v[212:215], v[64:67]\n"
    "v_mfma_f32_16x16x32_bf16 v[68:71], a[196:199], v[212:215], v[68:71]\n"
    "v_mfma_f32_16x16x32_bf16 v[72:75], a[200:203], v[212:215], v[72:75]\n"
    "v_mfma_f32_16x16x32_bf16 v[76:79], a[204:207], v[212:215], v[76:79]\n"
    "v_mfma_f32_16x16x32_bf16 v[80:83], a[208:211], v[212:215], v[80:83]\n"
    "v_mfma_f32_16x16x32_bf16 v[84:87], a[212:215], v[212:215], v[84:87]\n"
    "v_mfma_f32_16x16x32_bf16 v[88:91], a[216:219], v[212:215], v[88:91]\n"
    "v_mfma_f32_16x16x32_bf16 v[92:95], a[220:223], v[212:215], v[92:95]\n"
    "s_waitcnt lgkmcnt(3)\n"
    "v_lshlrev_b32 v112, 16, v96\n"
    "v_lshlrev_b32 v114, 16, v97\n"
    "v_lshlrev_b32 v116, 16, v98\n"
    "v_lshlrev_b32 v118, 16, v99\n"
    "v_and_b32 v113, s95, v96\n"
    "v_and_b32 v115, s95, v97\n"
    "v_and_b32 v117, s95, v98\n"
    "v_and_b32 v119, s95, v99\n"
    "v_pk_mul_f32 v[112:113], v[112:113], s[50:51]\n"
    "v_pk_mul_f32 v[114:115], v[114:115], s[50:51]\n"
    "v_pk_mul_f32 v[116:117], v[116:117], s[50:51]\n"
    "v_pk_mul_f32 v[118:119], v[118:119], s[50:51]\n"
    "v_cvt_pk_bf16_f32 v120, v112, v113\n"
    "v_cvt_pk_bf16_f32 v121, v114, v115\n"
    "v_cvt_pk_bf16_f32 v122, v116, v117\n"
    "v_cvt_pk_bf16_f32 v123, v118, v119\n"
    "v_accvgpr_write_b32 a48, v120\n"
    "v_accvgpr_write_b32 a49, v121\n"
    "v_accvgpr_write_b32 a50, v122\n"
    "v_accvgpr_write_b32 a51, v123\n"
    "s_waitcnt lgkmcnt(2)\n"
    "v_lshlrev_b32 v112, 16, v100\n"
    "v_lshlrev_b32 v114, 16, v101\n"
    "v_mfma_f32_16x16x32_bf16 v[192:195], a[96:99], a[48:51], 0\n"
    "v_lshlrev_b32 v116, 16, v102\n"
    "v_lshlrev_b32 v118, 16, v103\n"
    "v_and_b32 v113, s95, v100\n"
    "v_mfma_f32_16x16x32_bf16 v[196:199], a[112:115], a[48:51], 0\n"
    "v_and_b32 v115, s95, v101\n"
    "v_and_b32 v117, s95, v102\n"
    "v_and_b32 v119, s95, v103\n"
    "v_pk_mul_f32 v[112:113], v[112:113], s[50:51]\n"
    "v_pk_mul_f32 v[114:115], v[114:115], s[50:51]\n"
    "v_pk_mul_f32 v[116:117], v[116:117], s[50:51]\n"
    "v_pk_mul_f32 v[118:119], v[118:119], s[50:51]\n"
    "v_cvt_pk_bf16_f32 v120, v112, v113\n"
    "v_cvt_pk_bf16_f32 v121, v114, v115\n"
    "v_cvt_pk_bf16_f32 v122, v116, v117\n"
    "v_mfma_f32_16x16x32_bf16 v[200:203], a[128:131], a[48:51], 0\n"
    "v_cvt_pk_bf16_f32 v123, v118, v119\n"
    "v_accvgpr_write_b32 a52, v120\n"
    "v_accvgpr_write_b32 a53, v121\n"
    "v_accvgpr_write_b32 a54, v122\n"
    "v_mfma_f32_16x16x32_bf16 v[204:207], a[144:147], a[48:51], 0\n"
    "v_accvgpr_write_b32 a55, v123\n"
    "s_waitcnt lgkmcnt(1)\n"
    "v_lshlrev_b32 v112, 16, v104\n"
    "v_lshlrev_b32 v114, 16, v105\n"
    "v_mfma_f32_16x16x32_bf16 v[192:195], a[100:103], a[52:55], v[192:195]\n"
    "v_lshlrev_b32 v116, 16, v106\n"
    "v_lshlrev_b32 v118, 16, v107\n"
    "v_and_b32 v113, s95, v104\n"
    "v_mfma_f32_16x16x32_bf16 v[196:199], a[116:119], a[52:55], v[196:199]\n"
    "v_and_b32 v115, s95, v105\n"
    "v_and_b32 v117, s95, v106\n"
    "v_and_b32 v119, s95, v107\n"
    "v_pk_mul_f32 v[112:113], v[112:113], s[50:51]\n"
    "v_pk_mul_f32 v[114:115], v[114:115], s[50:51]\n"
    "v_pk_mul_f32 v[116:117], v[116:117], s[50:51]\n"
    "v_pk_mul_f32 v[118:119], v[118:119], s[50:51]\n"
    "v_cvt_pk_bf16_f32 v120, v112, v113\n"
    "v_cvt_pk_bf16_f32 v121, v114, v115\n"
    "v_cvt_pk_bf16_f32 v122, v116, v117\n"
    "v_mfma_f32_16x16x32_bf16 v[200:203], a[132:135], a[52:55], v[200:203]\n"
    "v_cvt_pk_bf16_f32 v123, v118, v119\n"
    "v_accvgpr_write_b32 a56, v120\n"
    "v_accvgpr_write_b32 a57, v121\n"
    "v_accvgpr_write_b32 a58, v122\n"
    "v_mfma_f32_16x16x32_bf16 v[204:207], a[148:151], a[52:55], v[204:207]\n"
    "v_accvgpr_write_b32 a59, v123\n"
    "s_waitcnt lgkmcnt(0)\n"
    "v_lshlrev_b32 v112, 16, v108\n"
    "v_lshlrev_b32 v114, 16, v109\n"
    "v_mfma_f32_16x16x32_bf16 v[192:195], a[104:107], a[56:59], v[192:195]\n"
    "v_lshlrev_b32 v116, 16, v110\n"
    "v_lshlrev_b32 v118, 16, v111\n"
    "v_and_b32 v113, s95, v108\n"
    "v_mfma_f32_16x16x32_bf16 v[196:199], a[120:123], a[56:59], v[196:199]\n"
    "v_and_b32 v115, s95, v109\n"
    "v_and_b32 v117, s95, v110\n"
    "v_and_b32 v119, s95, v111\n"
    "v_pk_mul_f32 v[112:113], v[112:113], s[50:51]\n"
    "v_pk_mul_f32 v[114:115], v[114:115], s[50:51]\n"
    "v_pk_mul_f32 v[116:117], v[116:117], s[50:51]\n"
    "v_pk_mul_f32 v[118:119], v[118:119], s[50:51]\n"
    "v_cvt_pk_bf16_f32 v120, v112, v113\n"
    "v_cvt_pk_bf16_f32 v121, v114, v115\n"
    "v_cvt_pk_bf16_f32 v122, v116, v117\n"
    "v_mfma_f32_16x16x32_bf16 v[200:203], a[136:139], a[56:59], v[200:203]\n"
    "v_cvt_pk_bf16_f32 v123, v118, v119\n"
    "v_accvgpr_write_b32 a60, v120\n"
    "v_accvgpr_write_b32 a61, v121\n"
    "v_accvgpr_write_b32 a62, v122\n"
    "v_mfma_f32_16x16x32_bf16 v[204:207], a[152:155], a[56:59], v[204:207]\n"
    "v_accvgpr_write_b32 a63, v123\n"
    "s_nop 1\n"
    "v_mfma_f32_16x16x32_bf16 v[192:195], a[108:111], a[60:63], v[192:195]\n"
    "v_mfma_f32_16x16x32_bf16 v[196:199], a[124:127], a[60:63], v[196:199]\n"
    "v_mfma_f32_16x16x32_bf16 v[200:203], a[140:143], a[60:63], v[200:203]\n"
    "v_mfma_f32_16x16x32_bf16 v[204:207], a[156:159], a[60:63], v[204:207]\n"
    "s_nop 4\n"
    "v_exp_f32 v192, v192\n"
    "v_exp_f32 v193, v193\n"
    "v_exp_f32 v194, v194\n"
    "v_exp_f32 v195, v195\n"
    "v_cvt_pk_bf16_f32 v208, v192, v193\n"
    "v_mov_b32 v219, v192\n"
    "v_exp_f32 v196, v196\n"
    "v_cvt_pk_bf16_f32 v209, v194, v195\n"
    "v_add_f32 v219, v219, v193\n"
    "v_exp_f32 v197, v197\n"
    "v_add_f32 v219, v219, v194\n"
    "v_exp_f32 v198, v198\n"
    "v_add_f32 v219, v219, v195\n"
    "v_exp_f32 v199, v199\n"
    "v_add_f32 v219, v219, v196\n"
    "v_cvt_pk_bf16_f32 v210, v196, v197\n"
    "v_add_f32 v219, v219, v197\n"
    "v_add_f32 v219, v219, v198\n"
    "v_cvt_pk_bf16_f32 v211, v198, v199\n"
    "v_add_f32 v219, v219, v199\n"
    "v_exp_f32 v200, v200\n"
    "v_exp_f32 v201, v201\n"
    "v_mfma_f32_16x16x32_bf16 v[96:99], a[160:163], v[208:211], 0\n"
    "v_exp_f32 v202, v202\n"
    "v_exp_f32 v203, v203\n"
    "v_mfma_f32_16x16x32_bf16 v[100:103], a[164:167], v[208:211], 0\n"
    "v_cvt_pk_bf16_f32 v212, v200, v201\n"
    "v_add_f32 v219, v219, v200\n"
    "v_mfma_f32_16x16x32_bf16 v[104:107], a[168:171], v[208:211], 0\n"
    "v_exp_f32 v204, v204\n"
    "v_cvt_pk_bf16_f32 v213, v202, v203\n"
    "v_mfma_f32_16x16x32_bf16 v[108:111], a[172:175], v[208:211], 0\n"
    "v_add_f32 v219, v219, v201\n"
    "v_exp_f32 v205, v205\n"
    "v_mfma_f32_16x16x32_bf16 v[112:115], a[176:179], v[208:211], 0\n"
    "v_add_f32 v219, v219, v202\n"
    "v_exp_f32 v206, v206\n"
    "v_mfma_f32_16x16x32_bf16 v[116:119], a[180:183], v[208:211], 0\n"
    "v_add_f32 v219, v219, v203\n"
    "v_exp_f32 v207, v207\n"
    "v_mfma_f32_16x16x32_bf16 v[120:123], a[184:187], v[208:211], 0\n"
    "v_add_f32 v219, v219, v204\n"
    "v_cvt_pk_bf16_f32 v214, v204, v205\n"
    "v_mfma_f32_16x16x32_bf16 v[124:127], a[188:191], v[208:211], 0\n"
    "v_add_f32 v219, v219, v205\n"
    "v_add_f32 v219, v219, v206\n"
    "v_cvt_pk_bf16_f32 v215, v206, v207\n"
    "v_add_f32 v219, v219, v207\n"
    "s_cmp_eq_u32 s59, 3\n"
    "s_cbranch_scc1 OWQ4%=\n"
    "s_waitcnt vmcnt(8)\n"
    "JWQ4%=:\n"
    "v_add_u32 v242, 147456, v235\n"
    "v_add_u32 v243, 147456, v236\n"
    "ds_read_b128 v[128:131], v242 offset:0\n"
    "ds_read_b128 v[132:135], v243 offset:0\n"
    "ds_read_b128 v[136:139], v242 offset:2048\n"
    "ds_read_b128 v[140:143], v243 offset:2048\n"
    "s_cmp_eq_u32 s59, 3\n"
    "s_cbranch_scc1 OI4%=\n"
    "JI4%=:\n"
    "DS4%=:\n"
    "v_mfma_f32_16x16x32_bf16 v[96:99], a[192:195], v[212:215], v[96:99]\n"
    "v_mfma_f32_16x16x32_bf16 v[100:103], a[196:199], v[212:215], v[100:103]\n"
    "v_mfma_f32_16x16x32_bf16 v[104:107], a[200:203], v[212:215], v[104:107]\n"
    "v_mfma_f32_16x16x32_bf16 v[108:111], a[204:207], v[212:215], v[108:111]\n"
    "v_mfma_f32_16x16x32_bf16 v[112:115], a[208:211], v[212:215], v[112:115]\n"
    "v_mfma_f32_16x16x32_bf16 v[116:119], a[212:215], v[212:215], v[116:119]\n"
    "v_mfma_f32_16x16x32_bf16 v[120:123], a[216:219], v[212:215], v[120:123]\n"
    "v_mfma_f32_16x16x32_bf16 v[124:127], a[220:223], v[212:215], v[124:127]\n"
    "s_waitcnt lgkmcnt(3)\n"
    "v_lshlrev_b32 v144, 16, v128\n"
    "v_lshlrev_b32 v146, 16, v129\n"
    "v_lshlrev_b32 v148, 16, v130\n"
    "v_lshlrev_b32 v150, 16, v131\n"
    "v_and_b32 v145, s95, v128\n"
    "v_and_b32 v147, s95, v129\n"
    "v_and_b32 v149, s95, v130\n"
    "v_and_b32 v151, s95, v131\n"
    "v_pk_mul_f32 v[144:145], v[144:145], s[50:51]\n"
    "v_pk_mul_f32 v[146:147], v[146:147], s[50:51]\n"
    "v_pk_mul_f32 v[148:149], v[148:149], s[50:51]\n"
    "v_pk_mul_f32 v[150:151], v[150:151], s[50:51]\n"
    "v_cvt_pk_bf16_f32 v152, v144, v145\n"
    "v_cvt_pk_bf16_f32 v153, v146, v147\n"
    "v_cvt_pk_bf16_f32 v154, v148, v149\n"
    "v_cvt_pk_bf16_f32 v155, v150, v151\n"
    "v_accvgpr_write_b32 a64, v152\n"
    "v_accvgpr_write_b32 a65, v153\n"
    "v_accvgpr_write_b32 a66, v154\n"
    "v_accvgpr_write_b32 a67, v155\n"
    "s_waitcnt lgkmcnt(2)\n"
    "v_lshlrev_b32 v144, 16, v132\n"
    "v_lshlrev_b32 v146, 16, v133\n"
    "v_mfma_f32_16x16x32_bf16 v[192:195], a[96:99], a[64:67], 0\n"
    "v_lshlrev_b32 v148, 16, v134\n"
    "v_lshlrev_b32 v150, 16, v135\n"
    "v_and_b32 v145, s95, v132\n"
    "v_mfma_f32_16x16x32_bf16 v[196:199], a[112:115], a[64:67], 0\n"
    "v_and_b32 v147, s95, v133\n"
    "v_and_b32 v149, s95, v134\n"
    "v_and_b32 v151, s95, v135\n"
    "v_pk_mul_f32 v[144:145], v[144:145], s[50:51]\n"
    "v_pk_mul_f32 v[146:147], v[146:147], s[50:51]\n"
    "v_pk_mul_f32 v[148:149], v[148:149], s[50:51]\n"
    "v_pk_mul_f32 v[150:151], v[150:151], s[50:51]\n"
    "v_cvt_pk_bf16_f32 v152, v144, v145\n"
    "v_cvt_pk_bf16_f32 v153, v146, v147\n"
    "v_cvt_pk_bf16_f32 v154, v148, v149\n"
    "v_mfma_f32_16x16x32_bf16 v[200:203], a[128:131], a[64:67], 0\n"
    "v_cvt_pk_bf16_f32 v155, v150, v151\n"
    "v_accvgpr_write_b32 a68, v152\n"
    "v_accvgpr_write_b32 a69, v153\n"
    "v_accvgpr_write_b32 a70, v154\n"
    "v_mfma_f32_16x16x32_bf16 v[204:207], a[144:147], a[64:67], 0\n"
    "v_accvgpr_write_b32 a71, v155\n"
    "s_waitcnt lgkmcnt(1)\n"
    "v_lshlrev_b32 v144, 16, v136\n"
    "v_lshlrev_b32 v146, 16, v137\n"
    "v_mfma_f32_16x16x32_bf16 v[192:195], a[100:103], a[68:71], v[192:195]\n"
    "v_lshlrev_b32 v148, 16, v138\n"
    "v_lshlrev_b32 v150, 16, v139\n"
    "v_and_b32 v145, s95, v136\n"
    "v_mfma_f32_16x16x32_bf16 v[196:199], a[116:119], a[68:71], v[196:199]\n"
    "v_and_b32 v147, s95, v137\n"
    "v_and_b32 v149, s95, v138\n"
    "v_and_b32 v151, s95, v139\n"
    "v_pk_mul_f32 v[144:145], v[144:145], s[50:51]\n"
    "v_pk_mul_f32 v[146:147], v[146:147], s[50:51]\n"
    "v_pk_mul_f32 v[148:149], v[148:149], s[50:51]\n"
    "v_pk_mul_f32 v[150:151], v[150:151], s[50:51]\n"
    "v_cvt_pk_bf16_f32 v152, v144, v145\n"
    "v_cvt_pk_bf16_f32 v153, v146, v147\n"
    "v_cvt_pk_bf16_f32 v154, v148, v149\n"
    "v_mfma_f32_16x16x32_bf16 v[200:203], a[132:135], a[68:71], v[200:203]\n"
    "v_cvt_pk_bf16_f32 v155, v150, v151\n"
    "v_accvgpr_write_b32 a72, v152\n"
    "v_accvgpr_write_b32 a73, v153\n"
    "v_accvgpr_write_b32 a74, v154\n"
    "v_mfma_f32_16x16x32_bf16 v[204:207], a[148:151], a[68:71], v[204:207]\n"
    "v_accvgpr_write_b32 a75, v155\n"
    "s_waitcnt lgkmcnt(0)\n"
    "v_lshlrev_b32 v144, 16, v140\n"
    "v_lshlrev_b32 v146, 16, v141\n"
    "v_mfma_f32_16x16x32_bf16 v[192:195], a[104:107], a[72:75], v[192:195]\n"
    "v_lshlrev_b32 v148, 16, v142\n"
    "v_lshlrev_b32 v150, 16, v143\n"
    "v_and_b32 v145, s95, v140\n"
    "v_mfma_f32_16x16x32_bf16 v[196:199], a[120:123], a[72:75], v[196:199]\n"
    "v_and_b32 v147, s95, v141\n"
    "v_and_b32 v149, s95, v142\n"
    "v_and_b32 v151, s95, v143\n"
    "v_pk_mul_f32 v[144:145], v[144:145], s[50:51]\n"
    "v_pk_mul_f32 v[146:147], v[146:147], s[50:51]\n"
    "v_pk_mul_f32 v[148:149], v[148:149], s[50:51]\n"
    "v_pk_mul_f32 v[150:151], v[150:151], s[50:51]\n"
    "v_cvt_pk_bf16_f32 v152, v144, v145\n"
    "v_cvt_pk_bf16_f32 v153, v146, v147\n"
    "v_cvt_pk_bf16_f32 v154, v148, v149\n"
    "v_mfma_f32_16x16x32_bf16 v[200:203], a[136:139], a[72:75], v[200:203]\n"
    "v_cvt_pk_bf16_f32 v155, v150, v151\n"
    "v_accvgpr_write_b32 a76, v152\n"
    "v_accvgpr_write_b32 a77, v153\n"
    "v_accvgpr_write_b32 a78, v154\n"
    "v_mfma_f32_16x16x32_bf16 v[204:207], a[152:155], a[72:75], v[204:207]\n"
    "v_accvgpr_write_b32 a79, v155\n"
    "s_nop 1\n"
    "v_mfma_f32_16x16x32_bf16 v[192:195], a[108:111], a[76:79], v[192:195]\n"
    "v_mfma_f32_16x16x32_bf16 v[196:199], a[124:127], a[76:79], v[196:199]\n"
    "v_mfma_f32_16x16x32_bf16 v[200:203], a[140:143], a[76:79], v[200:203]\n"
    "v_mfma_f32_16x16x32_bf16 v[204:207], a[156:159], a[76:79], v[204:207]\n"
    "s_nop 4\n"
    "v_exp_f32 v192, v192\n"
    "v_exp_f32 v193, v193\n"
    "v_exp_f32 v194, v194\n"
    "v_exp_f32 v195, v195\n"
    "v_cvt_pk_bf16_f32 v208, v192, v193\n"
    "v_mov_b32 v220, v192\n"
    "v_exp_f32 v196, v196\n"
    "v_cvt_pk_bf16_f32 v209, v194, v195\n"
    "v_add_f32 v220, v220, v193\n"
    "v_exp_f32 v197, v197\n"
    "v_add_f32 v220, v220, v194\n"
    "v_exp_f32 v198, v198\n"
    "v_add_f32 v220, v220, v195\n"
    "v_exp_f32 v199, v199\n"
    "v_add_f32 v220, v220, v196\n"
    "v_cvt_pk_bf16_f32 v210, v196, v197\n"
    "v_add_f32 v220, v220, v197\n"
    "v_add_f32 v220, v220, v198\n"
    "v_cvt_pk_bf16_f32 v211, v198, v199\n"
    "v_add_f32 v220, v220, v199\n"
    "v_exp_f32 v200, v200\n"
    "v_exp_f32 v201, v201\n"
    "v_mfma_f32_16x16x32_bf16 v[128:131], a[160:163], v[208:211], 0\n"
    "v_exp_f32 v202, v202\n"
    "v_exp_f32 v203, v203\n"
    "v_mfma_f32_16x16x32_bf16 v[132:135], a[164:167], v[208:211], 0\n"
    "v_cvt_pk_bf16_f32 v212, v200, v201\n"
    "v_add_f32 v220, v220, v200\n"
    "v_mfma_f32_16x16x32_bf16 v[136:139], a[168:171], v[208:211], 0\n"
    "v_exp_f32 v204, v204\n"
    "v_cvt_pk_bf16_f32 v213, v202, v203\n"
    "v_mfma_f32_16x16x32_bf16 v[140:143], a[172:175], v[208:211], 0\n"
    "v_add_f32 v220, v220, v201\n"
    "v_exp_f32 v205, v205\n"
    "v_mfma_f32_16x16x32_bf16 v[144:147], a[176:179], v[208:211], 0\n"
    "v_add_f32 v220, v220, v202\n"
    "v_exp_f32 v206, v206\n"
    "v_mfma_f32_16x16x32_bf16 v[148:151], a[180:183], v[208:211], 0\n"
    "v_add_f32 v220, v220, v203\n"
    "v_exp_f32 v207, v207\n"
    "v_mfma_f32_16x16x32_bf16 v[152:155], a[184:187], v[208:211], 0\n"
    "v_add_f32 v220, v220, v204\n"
    "v_cvt_pk_bf16_f32 v214, v204, v205\n"
    "v_mfma_f32_16x16x32_bf16 v[156:159], a[188:191], v[208:211], 0\n"
    "v_add_f32 v220, v220, v205\n"
    "v_add_f32 v220, v220, v206\n"
    "v_cvt_pk_bf16_f32 v215, v206, v207\n"
    "v_add_f32 v220, v220, v207\n"
    "s_cmp_eq_u32 s59, 3\n"
    "s_cbranch_scc1 OS5%=\n"
    "s_waitcnt vmcnt(4)\n"
    "v_add_u32 v242, 65536, v235\n"
    "v_add_u32 v243, 65536, v236\n"
    "ds_read_b128 v[160:163], v242 offset:0\n"
    "ds_read_b128 v[164:167], v243 offset:0\n"
    "ds_read_b128 v[168:171], v242 offset:2048\n"
    "ds_read_b128 v[172:175], v243 offset:2048\n"
    "DS5%=:\n"
    "v_mfma_f32_16x16x32_bf16 v[128:131], a[192:195], v[212:215], v[128:131]\n"
    "v_mfma_f32_16x16x32_bf16 v[132:135], a[196:199], v[212:215], v[132:135]\n"
    "v_mfma_f32_16x16x32_bf16 v[136:139], a[200:203], v[212:215], v[136:139]\n"
    "v_mfma_f32_16x16x32_bf16 v[140:143], a[204:207], v[212:215], v[140:143]\n"
    "v_mfma_f32_16x16x32_bf16 v[144:147], a[208:211], v[212:215], v[144:147]\n"
    "v_mfma_f32_16x16x32_bf16 v[148:151], a[212:215], v[212:215], v[148:151]\n"
    "v_mfma_f32_16x16x32_bf16 v[152:155], a[216:219], v[212:215], v[152:155]\n"
    "v_mfma_f32_16x16x32_bf16 v[156:159], a[220:223], v[212:215], v[156:159]\n"
    "s_waitcnt lgkmcnt(3)\n"
    "v_lshlrev_b32 v176, 16, v160\n"
    "v_lshlrev_b32 v178, 16, v161\n"
    "v_lshlrev_b32 v180, 16, v162\n"
    "v_lshlrev_b32 v182, 16, v163\n"
    "v_and_b32 v177, s95, v160\n"
    "v_and_b32 v179, s95, v161\n"
    "v_and_b32 v181, s95, v162\n"
    "v_and_b32 v183, s95, v163\n"
    "v_pk_mul_f32 v[176:177], v[176:177], s[50:51]\n"
    "v_pk_mul_f32 v[178:179], v[178:179], s[50:51]\n"
    "v_pk_mul_f32 v[180:181], v[180:181], s[50:51]\n"
    "v_pk_mul_f32 v[182:183], v[182:183], s[50:51]\n"
    "v_cvt_pk_bf16_f32 v184, v176, v177\n"
    "v_cvt_pk_bf16_f32 v185, v178, v179\n"
    "v_cvt_pk_bf16_f32 v186, v180, v181\n"
    "v_cvt_pk_bf16_f32 v187, v182, v183\n"
    "v_accvgpr_write_b32 a80, v184\n"
    "v_accvgpr_write_b32 a81, v185\n"
    "v_accvgpr_write_b32 a82, v186\n"
    "v_accvgpr_write_b32 a83, v187\n"
    "s_waitcnt lgkmcnt(2)\n"
    "v_lshlrev_b32 v176, 16, v164\n"
    "v_lshlrev_b32 v178, 16, v165\n"
    "v_mfma_f32_16x16x32_bf16 v[192:195], a[96:99], a[80:83], 0\n"
    "v_lshlrev_b32 v180, 16, v166\n"
    "v_lshlrev_b32 v182, 16, v167\n"
    "v_and_b32 v177, s95, v164\n"
    "v_mfma_f32_16x16x32_bf16 v[196:199], a[112:115], a[80:83], 0\n"
    "v_and_b32 v179, s95, v165\n"
    "v_and_b32 v181, s95, v166\n"
    "v_and_b32 v183, s95, v167\n"
    "v_pk_mul_f32 v[176:177], v[176:177], s[50:51]\n"
    "v_pk_mul_f32 v[178:179], v[178:179], s[50:51]\n"
    "v_pk_mul_f32 v[180:181], v[180:181], s[50:51]\n"
    "v_pk_mul_f32 v[182:183], v[182:183], s[50:51]\n"
    "v_cvt_pk_bf16_f32 v184, v176, v177\n"
    "v_cvt_pk_bf16_f32 v185, v178, v179\n"
    "v_cvt_pk_bf16_f32 v186, v180, v181\n"
    "v_mfma_f32_16x16x32_bf16 v[200:203], a[128:131], a[80:83], 0\n"
    "v_cvt_pk_bf16_f32 v187, v182, v183\n"
    "v_accvgpr_write_b32 a84, v184\n"
    "v_accvgpr_write_b32 a85, v185\n"
    "v_accvgpr_write_b32 a86, v186\n"
    "v_mfma_f32_16x16x32_bf16 v[204:207], a[144:147], a[80:83], 0\n"
    "v_accvgpr_write_b32 a87, v187\n"
    "s_waitcnt lgkmcnt(1)\n"
    "v_lshlrev_b32 v176, 16, v168\n"
    "v_lshlrev_b32 v178, 16, v169\n"
    "v_mfma_f32_16x16x32_bf16 v[192:195], a[100:103], a[84:87], v[192:195]\n"
    "v_lshlrev_b32 v180, 16, v170\n"
    "v_lshlrev_b32 v182, 16, v171\n"
    "v_and_b32 v177, s95, v168\n"
    "v_mfma_f32_16x16x32_bf16 v[196:199], a[116:119], a[84:87], v[196:199]\n"
    "v_and_b32 v179, s95, v169\n"
    "v_and_b32 v181, s95, v170\n"
    "v_and_b32 v183, s95, v171\n"
    "v_pk_mul_f32 v[176:177], v[176:177], s[50:51]\n"
    "v_pk_mul_f32 v[178:179], v[178:179], s[50:51]\n"
    "v_pk_mul_f32 v[180:181], v[180:181], s[50:51]\n"
    "v_pk_mul_f32 v[182:183], v[182:183], s[50:51]\n"
    "v_cvt_pk_bf16_f32 v184, v176, v177\n"
    "v_cvt_pk_bf16_f32 v185, v178, v179\n"
    "v_cvt_pk_bf16_f32 v186, v180, v181\n"
    "v_mfma_f32_16x16x32_bf16 v[200:203], a[132:135], a[84:87], v[200:203]\n"
    "v_cvt_pk_bf16_f32 v187, v182, v183\n"
    "v_accvgpr_write_b32 a88, v184\n"
    "v_accvgpr_write_b32 a89, v185\n"
    "v_accvgpr_write_b32 a90, v186\n"
    "v_mfma_f32_16x16x32_bf16 v[204:207], a[148:151], a[84:87], v[204:207]\n"
    "v_accvgpr_write_b32 a91, v187\n"
    "s_waitcnt lgkmcnt(0)\n"
    "v_lshlrev_b32 v176, 16, v172\n"
    "v_lshlrev_b32 v178, 16, v173\n"
    "v_mfma_f32_16x16x32_bf16 v[192:195], a[104:107], a[88:91], v[192:195]\n"
    "v_lshlrev_b32 v180, 16, v174\n"
    "v_lshlrev_b32 v182, 16, v175\n"
    "v_and_b32 v177, s95, v172\n"
    "v_mfma_f32_16x16x32_bf16 v[196:199], a[120:123], a[88:91], v[196:199]\n"
    "v_and_b32 v179, s95, v173\n"
    "v_and_b32 v181, s95, v174\n"
    "v_and_b32 v183, s95, v175\n"
    "v_pk_mul_f32 v[176:177], v[176:177], s[50:51]\n"
    "v_pk_mul_f32 v[178:179], v[178:179], s[50:51]\n"
    "v_pk_mul_f32 v[180:181], v[180:181], s[50:51]\n"
    "v_pk_mul_f32 v[182:183], v[182:183], s[50:51]\n"
    "v_cvt_pk_bf16_f32 v184, v176, v177\n"
    "v_cvt_pk_bf16_f32 v185, v178, v179\n"
    "v_cvt_pk_bf16_f32 v186, v180, v181\n"
    "v_mfma_f32_16x16x32_bf16 v[200:203], a[136:139], a[88:91], v[200:203]\n"
    "v_cvt_pk_bf16_f32 v187, v182, v183\n"
    "v_accvgpr_write_b32 a92, v184\n"
    "v_accvgpr_write_b32 a93, v185\n"
    "v_accvgpr_write_b32 a94, v186\n"
    "v_mfma_f32_16x16x32_bf16 v[204:207], a[152:155], a[88:91], v[204:207]\n"
    "v_accvgpr_write_b32 a95, v187\n"
    "s_nop 1\n"
    "v_mfma_f32_16x16x32_bf16 v[192:195], a[108:111], a[92:95], v[192:195]\n"
    "v_mfma_f32_16x16x32_bf16 v[196:199], a[124:127], a[92:95], v[196:199]\n"
    "v_mfma_f32_16x16x32_bf16 v[200:203], a[140:143], a[92:95], v[200:203]\n"
    "v_mfma_f32_16x16x32_bf16 v[204:207], a[156:159], a[92:95], v[204:207]\n"
    "s_nop 4\n"
    "v_exp_f32 v192, v192\n"
    "v_exp_f32 v193, v193\n"
    "v_exp_f32 v194, v194\n"
    "v_exp_f32 v195, v195\n"
    "v_cvt_pk_bf16_f32 v208, v192, v193\n"
    "v_mov_b32 v221, v192\n"
    "v_exp_f32 v196, v196\n"
    "v_cvt_pk_bf16_f32 v209, v194, v195\n"
    "v_add_f32 v221, v221, v193\n"
    "v_exp_f32 v197, v197\n"
    "v_add_f32 v221, v221, v194\n"
    "v_exp_f32 v198, v198\n"
    "v_add_f32 v221, v221, v195\n"
    "v_exp_f32 v199, v199\n"
    "v_add_f32 v221, v221, v196\n"
    "v_cvt_pk_bf16_f32 v210, v196, v197\n"
    "v_add_f32 v221, v221, v197\n"
    "v_add_f32 v221, v221, v198\n"
    "v_cvt_pk_bf16_f32 v211, v198, v199\n"
    "v_add_f32 v221, v221, v199\n"
    "v_exp_f32 v200, v200\n"
    "v_exp_f32 v201, v201\n"
    "v_mfma_f32_16x16x32_bf16 v[160:163], a[160:163], v[208:211], 0\n"
    "v_exp_f32 v202, v202\n"
    "v_exp_f32 v203, v203\n"
    "v_mfma_f32_16x16x32_bf16 v[164:167], a[164:167], v[208:211], 0\n"
    "v_cvt_pk_bf16_f32 v212, v200, v201\n"
    "v_add_f32 v221, v221, v200\n"
    "v_mfma_f32_16x16x32_bf16 v[168:171], a[168:171], v[208:211], 0\n"
    "v_exp_f32 v204, v204\n"
    "v_cvt_pk_bf16_f32 v213, v202, v203\n"
    "v_mfma_f32_16x16x32_bf16 v[172:175], a[172:175], v[208:211], 0\n"
    "v_add_f32 v221, v221, v201\n"
    "v_exp_f32 v205, v205\n"
    "v_mfma_f32_16x16x32_bf16 v[176:179], a[176:179], v[208:211], 0\n"
    "v_add_f32 v221, v221, v202\n"
    "v_exp_f32 v206, v206\n"
    "v_mfma_f32_16x16x32_bf16 v[180:183], a[180:183], v[208:211], 0\n"
    "v_add_f32 v221, v221, v203\n"
    "v_exp_f32 v207, v207\n"
    "v_mfma_f32_16x16x32_bf16 v[184:187], a[184:187], v[208:211], 0\n"
    "v_add_f32 v221, v221, v204\n"
    "v_cvt_pk_bf16_f32 v214, v204, v205\n"
    "v_mfma_f32_16x16x32_bf16 v[188:191], a[188:191], v[208:211], 0\n"
    "v_add_f32 v221, v221, v205\n"
    "v_add_f32 v221, v221, v206\n"
    "v_cvt_pk_bf16_f32 v215, v206, v207\n"
    "v_add_f32 v221, v221, v207\n"
    "s_nop 0\n"
    "v_mfma_f32_16x16x32_bf16 v[160:163], a[192:195], v[212:215], v[160:163]\n"
    "v_mfma_f32_16x16x32_bf16 v[164:167], a[196:199], v[212:215], v[164:167]\n"
    "v_mfma_f32_16x16x32_bf16 v[168:171], a[200:203], v[212:215], v[168:171]\n"
    "v_mfma_f32_16x16x32_bf16 v[172:175], a[204:207], v[212:215], v[172:175]\n"
    "v_mfma_f32_16x16x32_bf16 v[176:179], a[208:211], v[212:215], v[176:179]\n"
    "v_mfma_f32_16x16x32_bf16 v[180:183], a[212:215], v[212:215], v[180:183]\n"
    "v_mfma_f32_16x16x32_bf16 v[184:187], a[216:219], v[212:215], v[184:187]\n"
    "v_mfma_f32_16x16x32_bf16 v[188:191], a[220:223], v[212:215], v[188:191]\n"
    "JS5%=:\n"
    "s_cmp_eq_u32 s59, 3\n"
    "s_cbranch_scc1 OT2%=\n"
    "s_waitcnt vmcnt(0)\n"
    "JT2%=:\n"
    "s_barrier\n"
    "v_add_u32 v242, 114688, v237\n"
    "v_add_u32 v243, 114688, v238\n"
    "v_add_u32 v244, 114688, v239\n"
    "ds_read_b128 a[96:99], v242 offset:0\n"
    "ds_read_b128 a[100:103], v243 offset:0\n"
    "ds_read_b128 a[104:107], v242 offset:2048\n"
    "ds_read_b128 a[108:111], v243 offset:2048\n"
    "ds_read_b128 a[112:115], v242 offset:4096\n"
    "ds_read_b128 a[116:119], v243 offset:4096\n"
    "ds_read_b128 a[120:123], v242 offset:6144\n"
    "ds_read_b128 a[124:127], v243 offset:6144\n"
    "ds_read_b64_tr_b16 a[160:161], v244 offset:8192\n"
    "ds_read_b64_tr_b16 a[162:163], v244 offset:12288\n"
    "ds_read_b64_tr_b16 a[164:165], v244 offset:8448\n"
    "ds_read_b64_tr_b16 a[166:167], v244 offset:12544\n"
    "ds_read_b64_tr_b16 a[168:169], v244 offset:8704\n"
    "ds_read_b64_tr_b16 a[170:171], v244 offset:12800\n"
    "s_cmp_eq_u32 s59, 3\n"
    "s_cbranch_scc1 OF5%=\n"
    "JF5%=:\n"
    "s_mov_b32 s44, 2\n"
    "s_mov_b32 s46, 0\n"
    "s_mov_b32 s47, 49152\n"
    "s_mov_b32 s45, 16384\n"
    "s_mov_b32 s53, 0\n"
    "s_cmp_eq_u32 s59, 3\n"
    "s_cselect_b32 s53, 4096, s53\n"
    "s_add_u32 s48, s94, 49152\n"
    "s_and_b32 s48, s48, 131071\n"
    "s_waitcnt lgkmcnt(6)\n"
    "v_mfma_f32_16x16x32_bf16 v[192:195], a[96:99], a[0:3], 0\n"
    "ds_read_b64_tr_b16 a[172:173], v244 offset:8960\n"
    "v_mfma_f32_16x16x32_bf16 v[192:195], a[100:103], a[4:7], v[192:195]\n"
    "ds_read_b64_tr_b16 a[174:175], v244 offset:13056\n"
    "v_mfma_f32_16x16x32_bf16 v[192:195], a[104:107], a[8:11], v[192:195]\n"
    "ds_read_b64_tr_b16 a[176:177], v244 offset:9216\n"
    "v_mfma_f32_16x16x32_bf16 v[192:195], a[108:111], a[12:15], v[192:195]\n"
    "ds_read_b64_tr_b16 a[178:179], v244 offset:13312\n"
    "v_mfma_f32_16x16x32_bf16 v[196:199], a[112:115], a[0:3], 0\n"
    "ds_read_b64_tr_b16 a[180:181], v244 offset:9472\n"
    "v_mfma_f32_16x16x32_bf16 v[196:199], a[116:119], a[4:7], v[196:199]\n"
    "ds_read_b64_tr_b16 a[182:183], v244 offset:13568\n"
    "v_mfma_f32_16x16x32_bf16 v[196:199], a[120:123], a[8:11], v[196:199]\n"
    "ds_read_b64_tr_b16 a[184:185], v244 offset:9728\n"
    "v_mfma_f32_16x16x32_bf16 v[196:199], a[124:127], a[12:15], v[196:199]\n"
    "ds_read_b64_tr_b16 a[186:187], v244 offset:13824\n"
    "ds_read_b64_tr_b16 a[188:189], v244 offset:9984\n"
    "ds_read_b64_tr_b16 a[190:191], v244 offset:14080\n"
    "s_nop 3\n"
    "v_mfma_f32_16x16x32_bf16 v[200:203], a[96:99], a[16:19], 0\n"
    "v_exp_f32 v192, v192\n"
    "s_nop 0\n"
    "v_add_f32 v216, v216, v192\n"
    "v_mfma_f32_16x16x32_bf16 v[200:203], a[100:103], a[20:23], v[200:203]\n"
    "v_exp_f32 v193, v193\n"
    "s_nop 0\n"
    "v_add_f32 v216, v216, v193\n"
    "v_mfma_f32_16x16x32_bf16 v[200:203], a[104:107], a[24:27], v[200:203]\n"
    "v_exp_f32 v194, v194\n"
    "v_cvt_pk_bf16_f32 v208, v192, v193\n"
    "v_add_f32 v216, v216, v194\n"
    "v_mfma_f32_16x16x32_bf16 v[200:203], a[108:111], a[28:31], v[200:203]\n"
    "v_exp_f32 v195, v195\n"
    "s_nop 0\n"
    "v_add_f32 v216, v216, v195\n"
    "v_mfma_f32_16x16x32_bf16 v[204:207], a[112:115], a[16:19], 0\n"
    "v_exp_f32 v196, v196\n"
    "v_cvt_pk_bf16_f32 v209, v194, v195\n"
    "v_add_f32 v216, v216, v196\n"
    "v_mfma_f32_16x16x32_bf16 v[204:207], a[116:119], a[20:23], v[204:207]\n"
    "v_exp_f32 v197, v197\n"
    "s_nop 0\n"
    "v_add_f32 v216, v216, v197\n"
    "v_mfma_f32_16x16x32_bf16 v[204:207], a[120:123], a[24:27], v[204:207]\n"
    "v_exp_f32 v198, v198\n"
    "v_cvt_pk_bf16_f32 v210, v196, v197\n"
    "v_add_f32 v216, v216, v198\n"
    "v_mfma_f32_16x16x32_bf16 v[204:207], a[124:127], a[28:31], v[204:207]\n"
    "v_exp_f32 v199, v199\n"
    "s_nop 0\n"
    "v_add_f32 v216, v216, v199\n"
    "s_cmp_eq_u32 s59, 3\n"
    "s_cbranch_scc1 OT3%=\n"
    "JT3%=:\n"
    "s_barrier\n"
    "s_cmp_eq_u32 s59, 3\n"
    "s_cbranch_scc1 ENTB%=\n"
    "s_branch ENTA%=\n"
    "LOOPA%=:\n"
    "s_waitcnt lgkmcnt(8)\n"
    "v_mfma_f32_16x16x32_bf16 v[192:195], a[96:99], a[0:3], 0\n"
    "v_exp_f32 v200, v200\n"
    "v_mfma_f32_16x16x32_bf16 v[192:195], a[100:103], a[4:7], v[192:195]\n"
    "v_cvt_pk_bf16_f32 v211, v198, v199\n"
    "v_add_f32 v221, v221, v200\n"
    "v_mfma_f32_16x16x32_bf16 v[192:195], a[104:107], a[8:11], v[192:195]\n"
    "v_exp_f32 v201, v201\n"
    "v_mfma_f32_16x16x32_bf16 v[192:195], a[108:111], a[12:15], v[192:195]\n"
    "ds_read_b64_tr_b16 a[176:177], v224 offset:25600\n"
    "v_add_f32 v221, v221, v201\n"
    "v_mfma_f32_16x16x32_bf16 v[196:199], a[112:115], a[0:3], 0\n"
    "v_exp_f32 v202, v202\n"
    "v_mfma_f32_16x16x32_bf16 v[196:199], a[116:119], a[4:7], v[196:199]\n"
    "v_cvt_pk_bf16_f32 v212, v200, v201\n"
    "v_add_f32 v221, v221, v202\n"
    "v_mfma_f32_16x16x32_bf16 v[196:199], a[120:123], a[8:11], v[196:199]\n"
    "v_exp_f32 v203, v203\n"
    "v_mfma_f32_16x16x32_bf16 v[196:199], a[124:127], a[12:15], v[196:199]\n"
    "ds_read_b64_tr_b16 a[178:179], v224 offset:29696\n"
    "v_add_f32 v221, v221, v203\n"
    "v_mfma_f32_16x16x32_bf16 v[128:131], a[192:195], v[208:211], v[128:131]\n"
    "v_exp_f32 v204, v204\n"
    "v_mfma_f32_16x16x32_bf16 v[132:135], a[196:199], v[208:211], v[132:135]\n"
    "v_cvt_pk_bf16_f32 v213, v202, v203\n"
    "v_add_f32 v221, v221, v204\n"
    "v_mfma_f32_16x16x32_bf16 v[136:139], a[200:203], v[208:211], v[136:139]\n"
    "v_exp_f32 v205, v205\n"
    "v_mfma_f32_16x16x32_bf16 v[140:143], a[204:207], v[208:211], v[140:143]\n"
    "ds_read_b64_tr_b16 a[180:181], v224 offset:25856\n"
    "v_add_f32 v221, v221, v205\n"
    "v_mfma_f32_16x16x32_bf16 v[144:147], a[208:211], v[208:211], v[144:147]\n"
    "v_exp_f32 v206, v206\n"
    "v_mfma_f32_16x16x32_bf16 v[148:151], a[212:215], v[208:211], v[148:151]\n"
    "v_cvt_pk_bf16_f32 v214, v204, v205\n"
    "v_add_f32 v221, v221, v206\n"
    "v_mfma_f32_16x16x32_bf16 v[152:155], a[216:219], v[208:211], v[152:155]\n"
    "v_exp_f32 v207, v207\n"
    "v_mfma_f32_16x16x32_bf16 v[156:159], a[220:223], v[208:211], v[156:159]\n"
    "ds_read_b64_tr_b16 a[182:183], v224 offset:29952\n"
    "v_add_f32 v221, v221, v207\n"
    "v_mfma_f32_16x16x32_bf16 v[200:203], a[96:99], a[16:19], 0\n"
    "v_exp_f32 v192, v192\n"
    "v_mfma_f32_16x16x32_bf16 v[200:203], a[100:103], a[20:23], v[200:203]\n"
    "v_cvt_pk_bf16_f32 v215, v206, v207\n"
    "v_add_f32 v216, v216, v192\n"
    "v_mfma_f32_16x16x32_bf16 v[200:203], a[104:107], a[24:27], v[200:203]\n"
    "v_exp_f32 v193, v193\n"
    "v_mfma_f32_16x16x32_bf16 v[200:203], a[108:111], a[28:31], v[200:203]\n"
    "ds_read_b64_tr_b16 a[184:185], v224 offset:26112\n"
    "v_add_f32 v216, v216, v193\n"
    "v_mfma_f32_16x16x32_bf16 v[204:207], a[112:115], a[16:19], 0\n"
    "v_exp_f32 v194, v194\n"
    "v_mfma_f32_16x16x32_bf16 v[204:207], a[116:119], a[20:23], v[204:207]\n"
    "v_cvt_pk_bf16_f32 v208, v192, v193\n"
    "v_add_f32 v216, v216, v194\n"
    "v_mfma_f32_16x16x32_bf16 v[204:207], a[120:123], a[24:27], v[204:207]\n"
    "v_exp_f32 v195, v195\n"
    "v_mfma_f32_16x16x32_bf16 v[204:207], a[124:127], a[28:31], v[204:207]\n"
    "ds_read_b64_tr_b16 a[186:187], v224 offset:30208\n"
    "v_add_f32 v216, v216, v195\n"
    "v_mfma_f32_16x16x32_bf16 v[160:163], a[192:195], v[212:215], v[160:163]\n"
    "v_exp_f32 v196, v196\n"
    "v_mfma_f32_16x16x32_bf16 v[164:167], a[196:199], v[212:215], v[164:167]\n"
    "v_cvt_pk_bf16_f32 v209, v194, v195\n"
    "v_add_f32 v216, v216, v196\n"
    "v_mfma_f32_16x16x32_bf16 v[168:171], a[200:203], v[212:215], v[168:171]\n"
    "v_exp_f32 v197, v197\n"
    "v_mfma_f32_16x16x32_bf16 v[172:175], a[204:207], v[212:215], v[172:175]\n"
    "ds_read_b64_tr_b16 a[188:189], v224 offset:26368\n"
    "v_add_f32 v216, v216, v197\n"
    "v_mfma_f32_16x16x32_bf16 v[176:179], a[208:211], v[212:215], v[176:179]\n"
    "v_exp_f32 v198, v198\n"
    "v_mfma_f32_16x16x32_bf16 v[180:183], a[212:215], v[212:215], v[180:183]\n"
    "v_cvt_pk_bf16_f32 v210, v196, v197\n"
    "v_add_f32 v216, v216, v198\n"
    "v_mfma_f32_16x16x32_bf16 v[184:187], a[216:219], v[212:215], v[184:187]\n"
    "v_exp_f32 v199, v199\n"
    "v_mfma_f32_16x16x32_bf16 v[188:191], a[220:223], v[212:215], v[188:191]\n"
    "ds_read_b64_tr_b16 a[190:191], v224 offset:30464\n"
    "v_add_f32 v216, v216, v199\n"
    "v_add_u32 v222, s46, v237\n"
    "v_add_u32 v223, s46, v238\n"
    "v_add_u32 v224, s46, v239\n"
    "ENTA%=:\n"
    "v_mfma_f32_16x16x32_bf16 v[192:195], a[96:99], a[32:35], 0\n"
    "v_exp_f32 v200, v200\n"
    "v_mfma_f32_16x16x32_bf16 v[192:195], a[100:103], a[36:39], v[192:195]\n"
    "v_cvt_pk_bf16_f32 v211, v198, v199\n"
    "v_add_f32 v217, v217, v200\n"
    "v_mfma_f32_16x16x32_bf16 v[192:195], a[104:107], a[40:43], v[192:195]\n"
    "v_exp_f32 v201, v201\n"
    "v_mfma_f32_16x16x32_bf16 v[192:195], a[108:111], a[44:47], v[192:195]\n"
    "ds_read_b128 a[128:131], v222 offset:0\n"
    "v_add_f32 v217, v217, v201\n"
    "v_mfma_f32_16x16x32_bf16 v[196:199], a[112:115], a[32:35], 0\n"
    "v_exp_f32 v202, v202\n"
    "v_mfma_f32_16x16x32_bf16 v[196:199], a[116:119], a[36:39], v[196:199]\n"
    "v_cvt_pk_bf16_f32 v212, v200, v201\n"
    "v_add_f32 v217, v217, v202\n"
    "v_mfma_f32_16x16x32_bf16 v[196:199], a[120:123], a[40:43], v[196:199]\n"
    "v_exp_f32 v203, v203\n"
    "v_mfma_f32_16x16x32_bf16 v[196:199], a[124:127], a[44:47], v[196:199]\n"
    "ds_read_b128 a[132:135], v223 offset:0\n"
    "v_add_f32 v217, v217, v203\n"
    "s_waitcnt lgkmcnt(2)\n"
    "v_mfma_f32_16x16x32_bf16 v[0:3], a[160:163], v[208:211], v[0:3]\n"
    "v_exp_f32 v204, v204\n"
    "v_mfma_f32_16x16x32_bf16 v[4:7], a[164:167], v[208:211], v[4:7]\n"
    "v_cvt_pk_bf16_f32 v213, v202, v203\n"
    "v_add_f32 v217, v217, v204\n"
    "v_mfma_f32_16x16x32_bf16 v[8:11], a[168:171], v[208:211], v[8:11]\n"
    "v_exp_f32 v205, v205\n"
    "v_mfma_f32_16x16x32_bf16 v[12:15], a[172:175], v[208:211], v[12:15]\n"
    "ds_read_b128 a[136:139], v222 offset:2048\n"
    "v_add_f32 v217, v217, v205\n"
    "v_mfma_f32_16x16x32_bf16 v[16:19], a[176:179], v[208:211], v[16:19]\n"
    "v_exp_f32 v206, v206\n"
    "v_mfma_f32_16x16x32_bf16 v[20:23], a[180:183], v[208:211], v[20:23]\n"
    "v_cvt_pk_bf16_f32 v214, v204, v205\n"
    "v_add_f32 v217, v217, v206\n"
    "v_mfma_f32_16x16x32_bf16 v[24:27], a[184:187], v[208:211], v[24:27]\n"
    "v_exp_f32 v207, v207\n"
    "v_mfma_f32_16x16x32_bf16 v[28:31], a[188:191], v[208:211], v[28:31]\n"
    "ds_read_b128 a[140:143], v223 offset:2048\n"
    "v_add_f32 v217, v217, v207\n"
    "v_mfma_f32_16x16x32_bf16 v[200:203], a[96:99], a[48:51], 0\n"
    "v_exp_f32 v192, v192\n"
    "v_mfma_f32_16x16x32_bf16 v[200:203], a[100:103], a[52:55], v[200:203]\n"
    "v_cvt_pk_bf16_f32 v215, v206, v207\n"
    "v_add_f32 v218, v218, v192\n"
    "v_mfma_f32_16x16x32_bf16 v[200:203], a[104:107], a[56:59], v[200:203]\n"
    "v_exp_f32 v193, v193\n"
    "s_add_u32 s46, s46, 32768\n"
    "v_mfma_f32_16x16x32_bf16 v[200:203], a[108:111], a[60:63], v[200:203]\n"
    "ds_read_b128 a[144:147], v222 offset:4096\n"
    "v_add_f32 v218, v218, v193\n"
    "v_mfma_f32_16x16x32_bf16 v[204:207], a[112:115], a[48:51], 0\n"
    "v_exp_f32 v194, v194\n"
    "v_mfma_f32_16x16x32_bf16 v[204:207], a[116:119], a[52:55], v[204:207]\n"
    "v_cvt_pk_bf16_f32 v208, v192, v193\n"
    "v_add_f32 v218, v218, v194\n"
    "v_mfma_f32_16x16x32_bf16 v[204:207], a[120:123], a[56:59], v[204:207]\n"
    "v_exp_f32 v195, v195\n"
    "s_and_b32 s46, s46, 131071\n"
    "v_mfma_f32_16x16x32_bf16 v[204:207], a[124:127], a[60:63], v[204:207]\n"
    "ds_read_b128 a[148:151], v223 offset:4096\n"
    "v_add_f32 v218, v218, v195\n"
    "v_mfma_f32_16x16x32_bf16 v[32:35], a[160:163], v[212:215], v[32:35]\n"
    "v_exp_f32 v196, v196\n"
    "v_mfma_f32_16x16x32_bf16 v[36:39], a[164:167], v[212:215], v[36:39]\n"
    "v_cvt_pk_bf16_f32 v209, v194, v195\n"
    "v_add_f32 v218, v218, v196\n"
    "v_mfma_f32_16x16x32_bf16 v[40:43], a[168:171], v[212:215], v[40:43]\n"
    "v_exp_f32 v197, v197\n"
    "v_mfma_f32_16x16x32_bf16 v[44:47], a[172:175], v[212:215], v[44:47]\n"
    "ds_read_b128 a[152:155], v222 offset:6144\n"
    "v_add_f32 v218, v218, v197\n"
    "v_mfma_f32_16x16x32_bf16 v[48:51], a[176:179], v[212:215], v[48:51]\n"
    "v_exp_f32 v198, v198\n"
    "v_mfma_f32_16x16x32_bf16 v[52:55], a[180:183], v[212:215], v[52:55]\n"
    "v_cvt_pk_bf16_f32 v210, v196, v197\n"
    "v_add_f32 v218, v218, v198\n"
    "v_mfma_f32_16x16x32_bf16 v[56:59], a[184:187], v[212:215], v[56:59]\n"
    "v_exp_f32 v199, v199\n"
    "v_mfma_f32_16x16x32_bf16 v[60:63], a[188:191], v[212:215], v[60:63]\n"
    "ds_read_b128 a[156:159], v223 offset:6144\n"
    "v_add_f32 v218, v218, v199\n"
    "v_mfma_f32_16x16x32_bf16 v[192:195], a[96:99], a[64:67], 0\n"
    "v_exp_f32 v200, v200\n"
    "v_mfma_f32_16x16x32_bf16 v[192:195], a[100:103], a[68:71], v[192:195]\n"
    "v_cvt_pk_bf16_f32 v211, v198, v199\n"
    "v_add_f32 v219, v219, v200\n"
    "v_mfma_f32_16x16x32_bf16 v[192:195], a[104:107], a[72:75], v[192:195]\n"
    "v_exp_f32 v201, v201\n"
    "v_mfma_f32_16x16x32_bf16 v[192:195], a[108:111], a[76:79], v[192:195]\n"
    "ds_read_b64_tr_b16 a[192:193], v224 offset:8192\n"
    "v_add_f32 v219, v219, v201\n"
    "v_mfma_f32_16x16x32_bf16 v[196:199], a[112:115], a[64:67], 0\n"
    "v_exp_f32 v202, v202\n"
    "v_mfma_f32_16x16x32_bf16 v[196:199], a[116:119], a[68:71], v[196:199]\n"
    "v_cvt_pk_bf16_f32 v212, v200, v201\n"
    "v_add_f32 v219, v219, v202\n"
    "v_mfma_f32_16x16x32_bf16 v[196:199], a[120:123], a[72:75], v[196:199]\n"
    "v_exp_f32 v203, v203\n"
    "v_mfma_f32_16x16x32_bf16 v[196:199], a[124:127], a[76:79], v[196:199]\n"
    "ds_read_b64_tr_b16 a[194:195], v224 offset:12288\n"
    "v_add_f32 v219, v219, v203\n"
    "v_mfma_f32_16x16x32_bf16 v[64:67], a[160:163], v[208:211], v[64:67]\n"
    "v_exp_f32 v204, v204\n"
    "v_mfma_f32_16x16x32_bf16 v[68:71], a[164:167], v[208:211], v[68:71]\n"
    "v_cvt_pk_bf16_f32 v213, v202, v203\n"
    "v_add_f32 v219, v219, v204\n"
    "v_mfma_f32_16x16x32_bf16 v[72:75], a[168:171], v[208:211], v[72:75]\n"
    "v_exp_f32 v205, v205\n"
    "v_mfma_f32_16x16x32_bf16 v[76:79], a[172:175], v[208:211], v[76:79]\n"
    "ds_read_b64_tr_b16 a[196:197], v224 offset:8448\n"
    "v_add_f32 v219, v219, v205\n"
    "v_mfma_f32_16x16x32_bf16 v[80:83], a[176:179], v[208:211], v[80:83]\n"
    "v_exp_f32 v206, v206\n"
    "v_mfma_f32_16x16x32_bf16 v[84:87], a[180:183], v[208:211], v[84:87]\n"
    "v_cvt_pk_bf16_f32 v214, v204, v205\n"
    "v_add_f32 v219, v219, v206\n"
    "v_mfma_f32_16x16x32_bf16 v[88:91], a[184:187], v[208:211], v[88:91]\n"
    "v_exp_f32 v207, v207\n"
    "v_mfma_f32_16x16x32_bf16 v[92:95], a[188:191], v[208:211], v[92:95]\n"
    "ds_read_b64_tr_b16 a[198:199], v224 offset:12544\n"
    "v_add_f32 v219, v219, v207\n"
    "v_mfma_f32_16x16x32_bf16 v[200:203], a[96:99], a[80:83], 0\n"
    "v_exp_f32 v192, v192\n"
    "v_mfma_f32_16x16x32_bf16 v[200:203], a[100:103], a[84:87], v[200:203]\n"
    "v_cvt_pk_bf16_f32 v215, v206, v207\n"
    "v_add_f32 v220, v220, v192\n"
    "v_mfma_f32_16x16x32_bf16 v[200:203], a[104:107], a[88:91], v[200:203]\n"
    "v_exp_f32 v193, v193\n"
    "v_mfma_f32_16x16x32_bf16 v[200:203], a[108:111], a[92:95], v[200:203]\n"
    "ds_read_b64_tr_b16 a[200:201], v224 offset:8704\n"
    "v_add_f32 v220, v220, v193\n"
    "v_mfma_f32_16x16x32_bf16 v[204:207], a[112:115], a[80:83], 0\n"
    "v_exp_f32 v194, v194\n"
    "v_mfma_f32_16x16x32_bf16 v[204:207], a[116:119], a[84:87], v[204:207]\n"
    "v_cvt_pk_bf16_f32 v208, v192, v193\n"
    "v_add_f32 v220, v220, v194\n"
    "v_mfma_f32_16x16x32_bf16 v[204:207], a[120:123], a[88:91], v[204:207]\n"
    "v_exp_f32 v195, v195\n"
    "v_mfma_f32_16x16x32_bf16 v[204:207], a[124:127], a[92:95], v[204:207]\n"
    "ds_read_b64_tr_b16 a[202:203], v224 offset:12800\n"
    "v_add_f32 v220, v220, v195\n"
    "v_mfma_f32_16x16x32_bf16 v[96:99], a[160:163], v[212:215], v[96:99]\n"
    "v_exp_f32 v196, v196\n"
    "v_mfma_f32_16x16x32_bf16 v[100:103], a[164:167], v[212:215], v[100:103]\n"
    "v_cvt_pk_bf16_f32 v209, v194, v195\n"
    "v_add_f32 v220, v220, v196\n"
    "v_mfma_f32_16x16x32_bf16 v[104:107], a[168:171], v[212:215], v[104:107]\n"
    "v_exp_f32 v197, v197\n"
    "v_mfma_f32_16x16x32_bf16 v[108:111], a[172:175], v[212:215], v[108:111]\n"
    "ds_read_b64_tr_b16 a[204:205], v224 offset:8960\n"
    "v_add_f32 v220, v220, v197\n"
    "v_mfma_f32_16x16x32_bf16 v[112:115], a[176:179], v[212:215], v[112:115]\n"
    "v_exp_f32 v198, v198\n"
    "v_mfma_f32_16x16x32_bf16 v[116:119], a[180:183], v[212:215], v[116:119]\n"
    "v_cvt_pk_bf16_f32 v210, v196, v197\n"
    "v_add_f32 v220, v220, v198\n"
    "v_mfma_f32_16x16x32_bf16 v[120:123], a[184:187], v[212:215], v[120:123]\n"
    "v_exp_f32 v199, v199\n"
    "v_mfma_f32_16x16x32_bf16 v[124:127], a[188:191], v[212:215], v[124:127]\n"
    "ds_read_b64_tr_b16 a[206:207], v224 offset:13056\n"
    "v_add_f32 v220, v220, v199\n"
    "s_waitcnt lgkmcnt(8)\n"
    "v_mfma_f32_16x16x32_bf16 v[192:195], a[128:131], a[0:3], 0\n"
    "v_exp_f32 v200, v200\n"
    "v_mfma_f32_16x16x32_bf16 v[192:195], a[132:135], a[4:7], v[192:195]\n"
    "v_cvt_pk_bf16_f32 v211, v198, v199\n"
    "v_add_f32 v221, v221, v200\n"
    "v_mfma_f32_16x16x32_bf16 v[192:195], a[136:139], a[8:11], v[192:195]\n"
    "v_exp_f32 v201, v201\n"
    "v_mfma_f32_16x16x32_bf16 v[192:195], a[140:143], a[12:15], v[192:195]\n"
    "ds_read_b64_tr_b16 a[208:209], v224 offset:9216\n"
    "v_add_f32 v221, v221, v201\n"
    "v_mfma_f32_16x16x32_bf16 v[196:199], a[144:147], a[0:3], 0\n"
    "v_exp_f32 v202, v202\n"
    "v_mfma_f32_16x16x32_bf16 v[196:199], a[148:151], a[4:7], v[196:199]\n"
    "v_cvt_pk_bf16_f32 v212, v200, v201\n"
    "v_add_f32 v221, v221, v202\n"
    "v_mfma_f32_16x16x32_bf16 v[196:199], a[152:155], a[8:11], v[196:199]\n"
    "v_exp_f32 v203, v203\n"
    "v_mfma_f32_16x16x32_bf16 v[196:199], a[156:159], a[12:15], v[196:199]\n"
    "ds_read_b64_tr_b16 a[210:211], v224 offset:13312\n"
    "v_add_f32 v221, v221, v203\n"
    "v_mfma_f32_16x16x32_bf16 v[128:131], a[160:163], v[208:211], v[128:131]\n"
    "v_exp_f32 v204, v204\n"
    "v_mfma_f32_16x16x32_bf16 v[132:135], a[164:167], v[208:211], v[132:135]\n"
    "v_cvt_pk_bf16_f32 v213, v202, v203\n"
    "v_add_f32 v221, v221, v204\n"
    "v_mfma_f32_16x16x32_bf16 v[136:139], a[168:171], v[208:211], v[136:139]\n"
    "v_exp_f32 v205, v205\n"
    "v_mfma_f32_16x16x32_bf16 v[140:143], a[172:175], v[208:211], v[140:143]\n"
    "ds_read_b64_tr_b16 a[212:213], v224 offset:9472\n"
    "v_add_f32 v221, v221, v205\n"
    "v_mfma_f32_16x16x32_bf16 v[144:147], a[176:179], v[208:211], v[144:147]\n"
    "v_exp_f32 v206, v206\n"
    "v_mfma_f32_16x16x32_bf16 v[148:151], a[180:183], v[208:211], v[148:151]\n"
    "v_cvt_pk_bf16_f32 v214, v204, v205\n"
    "v_add_f32 v221, v221, v206\n"
    "v_mfma_f32_16x16x32_bf16 v[152:155], a[184:187], v[208:211], v[152:155]\n"
    "v_exp_f32 v207, v207\n"
    "v_mfma_f32_16x16x32_bf16 v[156:159], a[188:191], v[208:211], v[156:159]\n"
    "ds_read_b64_tr_b16 a[214:215], v224 offset:13568\n"
    "v_add_f32 v221, v221, v207\n"
    "v_mfma_f32_16x16x32_bf16 v[200:203], a[128:131], a[16:19], 0\n"
    "v_exp_f32 v192, v192\n"
    "v_mfma_f32_16x16x32_bf16 v[200:203], a[132:135], a[20:23], v[200:203]\n"
    "v_cvt_pk_bf16_f32 v215, v206, v207\n"
    "v_add_f32 v216, v216, v192\n"
    "v_mfma_f32_16x16x32_bf16 v[200:203], a[136:139], a[24:27], v[200:203]\n"
    "v_exp_f32 v193, v193\n"
    "v_mfma_f32_16x16x32_bf16 v[200:203], a[140:143], a[28:31], v[200:203]\n"
    "ds_read_b64_tr_b16 a[216:217], v224 offset:9728\n"
    "v_add_f32 v216, v216, v193\n"
    "v_mfma_f32_16x16x32_bf16 v[204:207], a[144:147], a[16:19], 0\n"
    "v_exp_f32 v194, v194\n"
    "v_mfma_f32_16x16x32_bf16 v[204:207], a[148:151], a[20:23], v[204:207]\n"
    "v_cvt_pk_bf16_f32 v208, v192, v193\n"
    "v_add_f32 v216, v216, v194\n"
    "v_mfma_f32_16x16x32_bf16 v[204:207], a[152:155], a[24:27], v[204:207]\n"
    "v_exp_f32 v195, v195\n"
    "v_mfma_f32_16x16x32_bf16 v[204:207], a[156:159], a[28:31], v[204:207]\n"
    "ds_read_b64_tr_b16 a[218:219], v224 offset:13824\n"
    "v_add_f32 v216, v216, v195\n"
    "v_mfma_f32_16x16x32_bf16 v[160:163], a[160:163], v[212:215], v[160:163]\n"
    "v_exp_f32 v196, v196\n"
    "v_mfma_f32_16x16x32_bf16 v[164:167], a[164:167], v[212:215], v[164:167]\n"
    "v_cvt_pk_bf16_f32 v209, v194, v195\n"
    "v_add_f32 v216, v216, v196\n"
    "v_mfma_f32_16x16x32_bf16 v[168:171], a[168:171], v[212:215], v[168:171]\n"
    "v_exp_f32 v197, v197\n"
    "v_mfma_f32_16x16x32_bf16 v[172:175], a[172:175], v[212:215], v[172:175]\n"
    "ds_read_b64_tr_b16 a[220:221], v224 offset:9984\n"
    "v_add_f32 v216, v216, v197\n"
    "v_mfma_f32_16x16x32_bf16 v[176:179], a[176:179], v[212:215], v[176:179]\n"
    "v_exp_f32 v198, v198\n"
    "v_mfma_f32_16x16x32_bf16 v[180:183], a[180:183], v[212:215], v[180:183]\n"
    "v_cvt_pk_bf16_f32 v210, v196, v197\n"
    "v_add_f32 v216, v216, v198\n"
    "v_mfma_f32_16x16x32_bf16 v[184:187], a[184:187], v[212:215], v[184:187]\n"
    "v_exp_f32 v199, v199\n"
    "v_mfma_f32_16x16x32_bf16 v[188:191], a[188:191], v[212:215], v[188:191]\n"
    "ds_read_b64_tr_b16 a[222:223], v224 offset:14080\n"
    "v_add_f32 v216, v216, v199\n"
    "s_barrier\n"
    "v_mfma_f32_16x16x32_bf16 v[192:195], a[128:131], a[32:35], 0\n"
    "v_exp_f32 v200, v200\n"
    "v_mfma_f32_16x16x32_bf16 v[192:195], a[132:135], a[36:39], v[192:195]\n"
    "v_cvt_pk_bf16_f32 v211, v198, v199\n"
    "v_add_f32 v217, v217, v200\n"
    "v_mfma_f32_16x16x32_bf16 v[192:195], a[136:139], a[40:43], v[192:195]\n"
    "v_exp_f32 v201, v201\n"
    "v_mfma_f32_16x16x32_bf16 v[192:195], a[140:143], a[44:47], v[192:195]\n"
    "ds_read_b128 a[96:99], v222 offset:16384\n"
    "v_add_f32 v217, v217, v201\n"
    "v_mfma_f32_16x16x32_bf16 v[196:199], a[144:147], a[32:35], 0\n"
    "v_exp_f32 v202, v202\n"
    "v_mfma_f32_16x16x32_bf16 v[196:199], a[148:151], a[36:39], v[196:199]\n"
    "v_cvt_pk_bf16_f32 v212, v200, v201\n"
    "v_add_f32 v217, v217, v202\n"
    "v_mfma_f32_16x16x32_bf16 v[196:199], a[152:155], a[40:43], v[196:199]\n"
    "v_exp_f32 v203, v203\n"
    "v_mfma_f32_16x16x32_bf16 v[196:199], a[156:159], a[44:47], v[196:199]\n"
    "ds_read_b128 a[100:103], v223 offset:16384\n"
    "v_add_f32 v217, v217, v203\n"
    "s_waitcnt lgkmcnt(2)\n"
    "v_mfma_f32_16x16x32_bf16 v[0:3], a[192:195], v[208:211], v[0:3]\n"
    "v_exp_f32 v204, v204\n"
    "v_mfma_f32_16x16x32_bf16 v[4:7], a[196:199], v[208:211], v[4:7]\n"
    "v_cvt_pk_bf16_f32 v213, v202, v203\n"
    "v_add_f32 v217, v217, v204\n"
    "v_mfma_f32_16x16x32_bf16 v[8:11], a[200:203], v[208:211], v[8:11]\n"
    "v_exp_f32 v205, v205\n"
    "v_mfma_f32_16x16x32_bf16 v[12:15], a[204:207], v[208:211], v[12:15]\n"
    "ds_read_b128 a[104:107], v222 offset:18432\n"
    "v_add_f32 v217, v217, v205\n"
    "v_mfma_f32_16x16x32_bf16 v[16:19], a[208:211], v[208:211], v[16:19]\n"
    "v_exp_f32 v206, v206\n"
    "v_mfma_f32_16x16x32_bf16 v[20:23], a[212:215], v[208:211], v[20:23]\n"
    "v_cvt_pk_bf16_f32 v214, v204, v205\n"
    "v_add_f32 v217, v217, v206\n"
    "v_mfma_f32_16x16x32_bf16 v[24:27], a[216:219], v[208:211], v[24:27]\n"
    "v_exp_f32 v207, v207\n"
    "v_mfma_f32_16x16x32_bf16 v[28:31], a[220:223], v[208:211], v[28:31]\n"
    "ds_read_b128 a[108:111], v223 offset:18432\n"
    "v_add_f32 v217, v217, v207\n"
    "v_mfma_f32_16x16x32_bf16 v[200:203], a[128:131], a[48:51], 0\n"
    "v_exp_f32 v192, v192\n"
    "v_mfma_f32_16x16x32_bf16 v[200:203], a[132:135], a[52:55], v[200:203]\n"
    "v_cvt_pk_bf16_f32 v215, v206, v207\n"
    "v_add_f32 v218, v218, v192\n"
    "v_mfma_f32_16x16x32_bf16 v[200:203], a[136:139], a[56:59], v[200:203]\n"
    "v_exp_f32 v193, v193\n"
    "s_add_u32 s44, s44, 2\n"
    "v_mfma_f32_16x16x32_bf16 v[200:203], a[140:143], a[60:63], v[200:203]\n"
    "ds_read_b128 a[112:115], v222 offset:20480\n"
    "v_add_f32 v218, v218, v193\n"
    "v_mfma_f32_16x16x32_bf16 v[204:207], a[144:147], a[48:51], 0\n"
    "v_exp_f32 v194, v194\n"
    "v_mfma_f32_16x16x32_bf16 v[204:207], a[148:151], a[52:55], v[204:207]\n"
    "v_cvt_pk_bf16_f32 v208, v192, v193\n"
    "v_add_f32 v218, v218, v194\n"
    "v_mfma_f32_16x16x32_bf16 v[204:207], a[152:155], a[56:59], v[204:207]\n"
    "v_exp_f32 v195, v195\n"
    "v_mfma_f32_16x16x32_bf16 v[204:207], a[156:159], a[60:63], v[204:207]\n"
    "ds_read_b128 a[116:119], v223 offset:20480\n"
    "v_add_f32 v218, v218, v195\n"
    "v_mfma_f32_16x16x32_bf16 v[32:35], a[192:195], v[212:215], v[32:35]\n"
    "v_exp_f32 v196, v196\n"
    "v_mfma_f32_16x16x32_bf16 v[36:39], a[196:199], v[212:215], v[36:39]\n"
    "v_cvt_pk_bf16_f32 v209, v194, v195\n"
    "v_add_f32 v218, v218, v196\n"
    "v_mfma_f32_16x16x32_bf16 v[40:43], a[200:203], v[212:215], v[40:43]\n"
    "v_exp_f32 v197, v197\n"
    "v_mfma_f32_16x16x32_bf16 v[44:47], a[204:207], v[212:215], v[44:47]\n"
    "ds_read_b128 a[120:123], v222 offset:22528\n"
    "v_add_f32 v218, v218, v197\n"
    "v_mfma_f32_16x16x32_bf16 v[48:51], a[208:211], v[212:215], v[48:51]\n"
    "v_exp_f32 v198, v198\n"
    "v_mfma_f32_16x16x32_bf16 v[52:55], a[212:215], v[212:215], v[52:55]\n"
    "v_cvt_pk_bf16_f32 v210, v196, v197\n"
    "v_add_f32 v218, v218, v198\n"
    "v_mfma_f32_16x16x32_bf16 v[56:59], a[216:219], v[212:215], v[56:59]\n"
    "v_exp_f32 v199, v199\n"
    "v_mfma_f32_16x16x32_bf16 v[60:63], a[220:223], v[212:215], v[60:63]\n"
    "ds_read_b128 a[124:127], v223 offset:22528\n"
    "v_add_f32 v218, v218, v199\n"
    "v_mfma_f32_16x16x32_bf16 v[192:195], a[128:131], a[64:67], 0\n"
    "v_exp_f32 v200, v200\n"
    "v_mfma_f32_16x16x32_bf16 v[192:195], a[132:135], a[68:71], v[192:195]\n"
    "v_cvt_pk_bf16_f32 v211, v198, v199\n"
    "v_add_f32 v219, v219, v200\n"
    "v_mfma_f32_16x16x32_bf16 v[192:195], a[136:139], a[72:75], v[192:195]\n"
    "v_exp_f32 v201, v201\n"
    "v_mfma_f32_16x16x32_bf16 v[192:195], a[140:143], a[76:79], v[192:195]\n"
    "ds_read_b64_tr_b16 a[160:161], v224 offset:24576\n"
    "v_add_f32 v219, v219, v201\n"
    "v_mfma_f32_16x16x32_bf16 v[196:199], a[144:147], a[64:67], 0\n"
    "v_exp_f32 v202, v202\n"
    "v_mfma_f32_16x16x32_bf16 v[196:199], a[148:151], a[68:71], v[196:199]\n"
    "v_cvt_pk_bf16_f32 v212, v200, v201\n"
    "v_add_f32 v219, v219, v202\n"
    "v_mfma_f32_16x16x32_bf16 v[196:199], a[152:155], a[72:75], v[196:199]\n"
    "v_exp_f32 v203, v203\n"
    "v_mfma_f32_16x16x32_bf16 v[196:199], a[156:159], a[76:79], v[196:199]\n"
    "ds_read_b64_tr_b16 a[162:163], v224 offset:28672\n"
    "v_add_f32 v219, v219, v203\n"
    "v_mfma_f32_16x16x32_bf16 v[64:67], a[192:195], v[208:211], v[64:67]\n"
    "v_exp_f32 v204, v204\n"
    "v_mfma_f32_16x16x32_bf16 v[68:71], a[196:199], v[208:211], v[68:71]\n"
    "v_cvt_pk_bf16_f32 v213, v202, v203\n"
    "v_add_f32 v219, v219, v204\n"
    "v_mfma_f32_16x16x32_bf16 v[72:75], a[200:203], v[208:211], v[72:75]\n"
    "v_exp_f32 v205, v205\n"
    "v_mfma_f32_16x16x32_bf16 v[76:79], a[204:207], v[208:211], v[76:79]\n"
    "ds_read_b64_tr_b16 a[164:165], v224 offset:24832\n"
    "v_add_f32 v219, v219, v205\n"
    "v_mfma_f32_16x16x32_bf16 v[80:83], a[208:211], v[208:211], v[80:83]\n"
    "v_exp_f32 v206, v206\n"
    "v_mfma_f32_16x16x32_bf16 v[84:87], a[212:215], v[208:211], v[84:87]\n"
    "v_cvt_pk_bf16_f32 v214, v204, v205\n"
    "v_add_f32 v219, v219, v206\n"
    "v_mfma_f32_16x16x32_bf16 v[88:91], a[216:219], v[208:211], v[88:91]\n"
    "v_exp_f32 v207, v207\n"
    "v_mfma_f32_16x16x32_bf16 v[92:95], a[220:223], v[208:211], v[92:95]\n"
    "ds_read_b64_tr_b16 a[166:167], v224 offset:28928\n"
    "v_add_f32 v219, v219, v207\n"
    "v_mfma_f32_16x16x32_bf16 v[200:203], a[128:131], a[80:83], 0\n"
    "v_exp_f32 v192, v192\n"
    "v_mfma_f32_16x16x32_bf16 v[200:203], a[132:135], a[84:87], v[200:203]\n"
    "v_cvt_pk_bf16_f32 v215, v206, v207\n"
    "v_add_f32 v220, v220, v192\n"
    "v_mfma_f32_16x16x32_bf16 v[200:203], a[136:139], a[88:91], v[200:203]\n"
    "v_exp_f32 v193, v193\n"
    "v_mfma_f32_16x16x32_bf16 v[200:203], a[140:143], a[92:95], v[200:203]\n"
    "ds_read_b64_tr_b16 a[168:169], v224 offset:25088\n"
    "v_add_f32 v220, v220, v193\n"
    "v_mfma_f32_16x16x32_bf16 v[204:207], a[144:147], a[80:83], 0\n"
    "v_exp_f32 v194, v194\n"
    "v_mfma_f32_16x16x32_bf16 v[204:207], a[148:151], a[84:87], v[204:207]\n"
    "v_cvt_pk_bf16_f32 v208, v192, v193\n"
    "v_add_f32 v220, v220, v194\n"
    "v_mfma_f32_16x16x32_bf16 v[204:207], a[152:155], a[88:91], v[204:207]\n"
    "v_exp_f32 v195, v195\n"
    "v_mfma_f32_16x16x32_bf16 v[204:207], a[156:159], a[92:95], v[204:207]\n"
    "ds_read_b64_tr_b16 a[170:171], v224 offset:29184\n"
    "v_add_f32 v220, v220, v195\n"
    "v_mfma_f32_16x16x32_bf16 v[96:99], a[192:195], v[212:215], v[96:99]\n"
    "v_exp_f32 v196, v196\n"
    "v_mfma_f32_16x16x32_bf16 v[100:103], a[196:199], v[212:215], v[100:103]\n"
    "v_cvt_pk_bf16_f32 v209, v194, v195\n"
    "v_add_f32 v220, v220, v196\n"
    "v_mfma_f32_16x16x32_bf16 v[104:107], a[200:203], v[212:215], v[104:107]\n"
    "v_exp_f32 v197, v197\n"
    "v_mfma_f32_16x16x32_bf16 v[108:111], a[204:207], v[212:215], v[108:111]\n"
    "ds_read_b64_tr_b16 a[172:173], v224 offset:25344\n"
    "v_add_f32 v220, v220, v197\n"
    "v_mfma_f32_16x16x32_bf16 v[112:115], a[208:211], v[212:215], v[112:115]\n"
    "v_exp_f32 v198, v198\n"
    "v_mfma_f32_16x16x32_bf16 v[116:119], a[212:215], v[212:215], v[116:119]\n"
    "v_cvt_pk_bf16_f32 v210, v196, v197\n"
    "v_add_f32 v220, v220, v198\n"
    "v_mfma_f32_16x16x32_bf16 v[120:123], a[216:219], v[212:215], v[120:123]\n"
    "v_exp_f32 v199, v199\n"
    "v_mfma_f32_16x16x32_bf16 v[124:127], a[220:223], v[212:215], v[124:127]\n"
    "ds_read_b64_tr_b16 a[174:175], v224 offset:29440\n"
    "v_add_f32 v220, v220, v199\n"
    "s_cmp_eq_u32 s44, 14\n"
    "s_cbranch_scc0 LOOPA%=\n"
    "TRA%=:\n"
    "s_barrier\n"
    "s_waitcnt lgkmcnt(8)\n"
    "v_mfma_f32_16x16x32_bf16 v[192:195], a[96:99], a[0:3], 0\n"
    "v_exp_f32 v200, v200\n"
    "v_mfma_f32_16x16x32_bf16 v[192:195], a[100:103], a[4:7], v[192:195]\n"
    "v_cvt_pk_bf16_f32 v211, v198, v199\n"
    "v_add_f32 v221, v221, v200\n"
    "ds_read_b128 a[128:131], v222 offset:32768\n"
    "v_mfma_f32_16x16x32_bf16 v[192:195], a[104:107], a[8:11], v[192:195]\n"
    "v_exp_f32 v201, v201\n"
    "v_mfma_f32_16x16x32_bf16 v[192:195], a[108:111], a[12:15], v[192:195]\n"
    "v_add_f32 v221, v221, v201\n"
    "ds_read_b128 a[132:135], v223 offset:32768\n"
    "ds_read_b128 a[136:139], v222 offset:34816\n"
    "v_mfma_f32_16x16x32_bf16 v[196:199], a[112:115], a[0:3], 0\n"
    "v_exp_f32 v202, v202\n"
    "v_mfma_f32_16x16x32_bf16 v[196:199], a[116:119], a[4:7], v[196:199]\n"
    "v_cvt_pk_bf16_f32 v212, v200, v201\n"
    "v_add_f32 v221, v221, v202\n"
    "ds_read_b128 a[140:143], v223 offset:34816\n"
    "v_mfma_f32_16x16x32_bf16 v[196:199], a[120:123], a[8:11], v[196:199]\n"
    "v_exp_f32 v203, v203\n"
    "v_mfma_f32_16x16x32_bf16 v[196:199], a[124:127], a[12:15], v[196:199]\n"
    "v_add_f32 v221, v221, v203\n"
    "ds_read_b128 a[144:147], v222 offset:36864\n"
    "ds_read_b128 a[148:151], v223 offset:36864\n"
    "v_mfma_f32_16x16x32_bf16 v[128:131], a[192:195], v[208:211], v[128:131]\n"
    "v_exp_f32 v204, v204\n"
    "v_mfma_f32_16x16x32_bf16 v[132:135], a[196:199], v[208:211], v[132:135]\n"
    "v_cvt_pk_bf16_f32 v213, v202, v203\n"
    "v_add_f32 v221, v221, v204\n"
    "ds_read_b128 a[152:155], v222 offset:38912\n"
    "v_mfma_f32_16x16x32_bf16 v[136:139], a[200:203], v[208:211], v[136:139]\n"
    "v_exp_f32 v205, v205\n"
    "v_mfma_f32_16x16x32_bf16 v[140:143], a[204:207], v[208:211], v[140:143]\n"
    "v_add_f32 v221, v221, v205\n"
    "ds_read_b128 a[156:159], v223 offset:38912\n"
    "ds_read_b64_tr_b16 a[176:177], v224 offset:25600\n"
    "v_mfma_f32_16x16x32_bf16 v[144:147], a[208:211], v[208:211], v[144:147]\n"
    "v_exp_f32 v206, v206\n"
    "v_mfma_f32_16x16x32_bf16 v[148:151], a[212:215], v[208:211], v[148:151]\n"
    "v_cvt_pk_bf16_f32 v214, v204, v205\n"
    "v_add_f32 v221, v221, v206\n"
    "ds_read_b64_tr_b16 a[178:179], v224 offset:29696\n"
    "v_mfma_f32_16x16x32_bf16 v[152:155], a[216:219], v[208:211], v[152:155]\n"
    "v_exp_f32 v207, v207\n"
    "v_mfma_f32_16x16x32_bf16 v[156:159], a[220:223], v[208:211], v[156:159]\n"
    "v_add_f32 v221, v221, v207\n"
    "ds_read_b64_tr_b16 a[180:181], v224 offset:25856\n"
    "ds_read_b64_tr_b16 a[182:183], v224 offset:29952\n"
    "s_waitcnt lgkmcnt(4)\n"
    "v_mfma_f32_16x16x32_bf16 v[200:203], a[128:131], a[0:3], 0\n"
    "v_exp_f32 v192, v192\n"
    "v_mfma_f32_16x16x32_bf16 v[200:203], a[132:135], a[4:7], v[200:203]\n"
    "v_cvt_pk_bf16_f32 v215, v206, v207\n"
    "v_add_f32 v216, v216, v192\n"
    "v_mfma_f32_16x16x32_bf16 v[200:203], a[136:139], a[8:11], v[200:203]\n"
    "v_exp_f32 v193, v193\n"
    "v_mfma_f32_16x16x32_bf16 v[200:203], a[140:143], a[12:15], v[200:203]\n"
    "ds_read_b64_tr_b16 a[184:185], v224 offset:26112\n"
    "v_add_f32 v216, v216, v193\n"
    "v_mfma_f32_16x16x32_bf16 v[204:207], a[144:147], a[0:3], 0\n"
    "v_exp_f32 v194, v194\n"
    "v_mfma_f32_16x16x32_bf16 v[204:207], a[148:151], a[4:7], v[204:207]\n"
    "v_cvt_pk_bf16_f32 v208, v192, v193\n"
    "v_add_f32 v216, v216, v194\n"
    "v_mfma_f32_16x16x32_bf16 v[204:207], a[152:155], a[8:11], v[204:207]\n"
    "v_exp_f32 v195, v195\n"
    "v_mfma_f32_16x16x32_bf16 v[204:207], a[156:159], a[12:15], v[204:207]\n"
    "ds_read_b64_tr_b16 a[186:187], v224 offset:30208\n"
    "v_add_f32 v216, v216, v195\n"
    "v_mfma_f32_16x16x32_bf16 v[160:163], a[192:195], v[212:215], v[160:163]\n"
    "v_exp_f32 v196, v196\n"
    "v_mfma_f32_16x16x32_bf16 v[164:167], a[196:199], v[212:215], v[164:167]\n"
    "v_cvt_pk_bf16_f32 v209, v194, v195\n"
    "v_add_f32 v216, v216, v196\n"
    "v_mfma_f32_16x16x32_bf16 v[168:171], a[200:203], v[212:215], v[168:171]\n"
    "v_exp_f32 v197, v197\n"
    "v_mfma_f32_16x16x32_bf16 v[172:175], a[204:207], v[212:215], v[172:175]\n"
    "ds_read_b64_tr_b16 a[188:189], v224 offset:26368\n"
    "v_add_f32 v216, v216, v197\n"
    "v_mfma_f32_16x16x32_bf16 v[176:179], a[208:211], v[212:215], v[176:179]\n"
    "v_exp_f32 v198, v198\n"
    "v_mfma_f32_16x16x32_bf16 v[180:183], a[212:215], v[212:215], v[180:183]\n"
    "v_cvt_pk_bf16_f32 v210, v196, v197\n"
    "v_add_f32 v216, v216, v198\n"
    "v_mfma_f32_16x16x32_bf16 v[184:187], a[216:219], v[212:215], v[184:187]\n"
    "v_exp_f32 v199, v199\n"
    "v_mfma_f32_16x16x32_bf16 v[188:191], a[220:223], v[212:215], v[188:191]\n"
    "ds_read_b64_tr_b16 a[190:191], v224 offset:30464\n"
    "v_add_f32 v216, v216, v199\n"
    "FINAL%=:\n"
    "DF0%=:\n"
    "v_mfma_f32_16x16x32_bf16 v[192:195], a[96:99], a[16:19], 0\n"
    "v_exp_f32 v200, v200\n"
    "v_lshlrev_b32 v229, 4, v255\n"
    "v_mfma_f32_16x16x32_bf16 v[192:195], a[100:103], a[20:23], v[192:195]\n"
    "v_cvt_pk_bf16_f32 v211, v198, v199\n"
    "v_add_f32 v216, v216, v200\n"
    "ds_read_b64_tr_b16 a[206:207], v224 offset:45824\n"
    "ds_read_b64_tr_b16 a[222:223], v224 offset:46848\n"
    "v_mfma_f32_16x16x32_bf16 v[192:195], a[104:107], a[24:27], v[192:195]\n"
    "v_exp_f32 v201, v201\n"
    "v_mfma_f32_16x16x32_bf16 v[192:195], a[108:111], a[28:31], v[192:195]\n"
    "ds_read_b64_tr_b16 a[192:193], v224 offset:40960\n"
    "v_add_f32 v216, v216, v201\n"
    "ds_read_b64_tr_b16 a[214:215], v224 offset:46336\n"
    "v_mfma_f32_16x16x32_bf16 v[196:199], a[112:115], a[16:19], 0\n"
    "v_exp_f32 v202, v202\n"
    "v_mfma_f32_16x16x32_bf16 v[196:199], a[116:119], a[20:23], v[196:199]\n"
    "v_cvt_pk_bf16_f32 v212, v200, v201\n"
    "v_add_f32 v216, v216, v202\n"
    "ds_read_b64_tr_b16 a[204:205], v224 offset:41728\n"
    "ds_read_b64_tr_b16 a[220:221], v224 offset:42752\n"
    "v_mfma_f32_16x16x32_bf16 v[196:199], a[120:123], a[24:27], v[196:199]\n"
    "v_exp_f32 v203, v203\n"
    "v_mfma_f32_16x16x32_bf16 v[196:199], a[124:127], a[28:31], v[196:199]\n"
    "ds_read_b64_tr_b16 a[194:195], v224 offset:45056\n"
    "v_add_f32 v216, v216, v203\n"
    "ds_read_b64_tr_b16 a[212:213], v224 offset:42240\n"
    "s_waitcnt lgkmcnt(8)\n"
    "v_mfma_f32_16x16x32_bf16 v[0:3], a[160:163], v[208:211], v[0:3]\n"
    "v_exp_f32 v204, v204\n"
    "v_mfma_f32_16x16x32_bf16 v[4:7], a[164:167], v[208:211], v[4:7]\n"
    "v_cvt_pk_bf16_f32 v213, v202, v203\n"
    "v_add_f32 v216, v216, v204\n"
    "ds_read_b64_tr_b16 a[200:201], v224 offset:41472\n"
    "ds_read_b64_tr_b16 a[216:217], v224 offset:42496\n"
    "v_mfma_f32_16x16x32_bf16 v[8:11], a[168:171], v[208:211], v[8:11]\n"
    "v_exp_f32 v205, v205\n"
    "v_mfma_f32_16x16x32_bf16 v[12:15], a[172:175], v[208:211], v[12:15]\n"
    "ds_read_b64_tr_b16 a[196:197], v224 offset:41216\n"
    "v_add_f32 v216, v216, v205\n"
    "ds_read_b64_tr_b16 a[208:209], v224 offset:41984\n"
    "v_mfma_f32_16x16x32_bf16 v[16:19], a[176:179], v[208:211], v[16:19]\n"
    "v_exp_f32 v206, v206\n"
    "v_mfma_f32_16x16x32_bf16 v[20:23], a[180:183], v[208:211], v[20:23]\n"
    "v_cvt_pk_bf16_f32 v214, v204, v205\n"
    "v_add_f32 v216, v216, v206\n"
    "ds_read_b64_tr_b16 a[202:203], v224 offset:45568\n"
    "ds_read_b64_tr_b16 a[218:219], v224 offset:46592\n"
    "v_mfma_f32_16x16x32_bf16 v[24:27], a[184:187], v[208:211], v[24:27]\n"
    "v_exp_f32 v207, v207\n"
    "v_mfma_f32_16x16x32_bf16 v[28:31], a[188:191], v[208:211], v[28:31]\n"
    "ds_read_b64_tr_b16 a[198:199], v224 offset:45312\n"
    "v_add_f32 v216, v216, v207\n"
    "ds_read_b64_tr_b16 a[210:211], v224 offset:46080\n"
    "v_mfma_f32_16x16x32_bf16 v[200:203], a[128:131], a[16:19], 0\n"
    "v_exp_f32 v192, v192\n"
    "v_mov_b32 v235, v216\n"
    "v_mfma_f32_16x16x32_bf16 v[200:203], a[132:135], a[20:23], v[200:203]\n"
    "v_cvt_pk_bf16_f32 v215, v206, v207\n"
    "v_add_f32 v217, v217, v192\n"
    "v_mfma_f32_16x16x32_bf16 v[200:203], a[136:139], a[24:27], v[200:203]\n"
    "v_exp_f32 v193, v193\n"
    "v_permlane16_swap_b32 v216, v235\n"
    "v_mfma_f32_16x16x32_bf16 v[200:203], a[140:143], a[28:31], v[200:203]\n"
    "v_add_f32 v217, v217, v193\n"
    "v_mfma_f32_16x16x32_bf16 v[204:207], a[144:147], a[16:19], 0\n"
    "v_exp_f32 v194, v194\n"
    "v_add_f32 v216, v216, v235\n"
    "v_mfma_f32_16x16x32_bf16 v[204:207], a[148:151], a[20:23], v[204:207]\n"
    "v_cvt_pk_bf16_f32 v208, v192, v193\n"
    "v_add_f32 v217, v217, v194\n"
    "v_mfma_f32_16x16x32_bf16 v[204:207], a[152:155], a[24:27], v[204:207]\n"
    "v_exp_f32 v195, v195\n"
    "v_mov_b32 v235, v216\n"
    "v_mfma_f32_16x16x32_bf16 v[204:207], a[156:159], a[28:31], v[204:207]\n"
    "v_add_f32 v217, v217, v195\n"
    "s_waitcnt lgkmcnt(0)\n"
    "v_mfma_f32_16x16x32_bf16 v[0:3], a[192:195], v[212:215], v[0:3]\n"
    "v_exp_f32 v196, v196\n"
    "v_permlane32_swap_b32 v216, v235\n"
    "v_mfma_f32_16x16x32_bf16 v[4:7], a[196:199], v[212:215], v[4:7]\n"
    "v_cvt_pk_bf16_f32 v209, v194, v195\n"
    "v_add_f32 v217, v217, v196\n"
    "v_mfma_f32_16x16x32_bf16 v[8:11], a[200:203], v[212:215], v[8:11]\n"
    "v_exp_f32 v197, v197\n"
    "v_add_f32 v216, v216, v235\n"
    "v_mfma_f32_16x16x32_bf16 v[12:15], a[204:207], v[212:215], v[12:15]\n"
    "v_add_f32 v217, v217, v197\n"
    "v_mfma_f32_16x16x32_bf16 v[16:19], a[208:211], v[212:215], v[16:19]\n"
    "v_exp_f32 v198, v198\n"
    "v_rcp_f32 v230, v216\n"
    "v_mfma_f32_16x16x32_bf16 v[20:23], a[212:215], v[212:215], v[20:23]\n"
    "v_cvt_pk_bf16_f32 v210, v196, v197\n"
    "v_add_f32 v217, v217, v198\n"
    "v_mfma_f32_16x16x32_bf16 v[24:27], a[216:219], v[212:215], v[24:27]\n"
    "v_exp_f32 v199, v199\n"
    "v_max_f32 v240, v216, v230\n"
    "v_mfma_f32_16x16x32_bf16 v[28:31], a[220:223], v[212:215], v[28:31]\n"
    "v_add_f32 v217, v217, v199\n"
    "DF1%=:\n"
    "v_mfma_f32_16x16x32_bf16 v[192:195], a[96:99], a[32:35], 0\n"
    "v_exp_f32 v200, v200\n"
    "v_pk_mul_f32 v[0:1], v[0:1], v[230:231] op_sel_hi:[1,0]\n"
    "v_pk_mul_f32 v[2:3], v[2:3], v[230:231] op_sel_hi:[1,0]\n"
    "v_pk_mul_f32 v[4:5], v[4:5], v[230:231] op_sel_hi:[1,0]\n"
    "v_pk_mul_f32 v[6:7], v[6:7], v[230:231] op_sel_hi:[1,0]\n"
    "v_pk_mul_f32 v[8:9], v[8:9], v[230:231] op_sel_hi:[1,0]\n"
    "v_pk_mul_f32 v[10:11], v[10:11], v[230:231] op_sel_hi:[1,0]\n"
    "v_pk_mul_f32 v[12:13], v[12:13], v[230:231] op_sel_hi:[1,0]\n"
    "v_pk_mul_f32 v[14:15], v[14:15], v[230:231] op_sel_hi:[1,0]\n"
    "v_pk_mul_f32 v[16:17], v[16:17], v[230:231] op_sel_hi:[1,0]\n"
    "v_pk_mul_f32 v[18:19], v[18:19], v[230:231] op_sel_hi:[1,0]\n"
    "v_pk_mul_f32 v[20:21], v[20:21], v[230:231] op_sel_hi:[1,0]\n"
    "v_pk_mul_f32 v[22:23], v[22:23], v[230:231] op_sel_hi:[1,0]\n"
    "v_pk_mul_f32 v[24:25], v[24:25], v[230:231] op_sel_hi:[1,0]\n"
    "v_pk_mul_f32 v[26:27], v[26:27], v[230:231] op_sel_hi:[1,0]\n"
    "v_pk_mul_f32 v[28:29], v[28:29], v[230:231] op_sel_hi:[1,0]\n"
    "v_pk_mul_f32 v[30:31], v[30:31], v[230:231] op_sel_hi:[1,0]\n"
    "v_mfma_f32_16x16x32_bf16 v[192:195], a[100:103], a[36:39], v[192:195]\n"
    "v_cvt_pk_bf16_f32 v211, v198, v199\n"
    "v_add_f32 v217, v217, v200\n"
    "v_cvt_pk_bf16_f32 v0, v0, v1\n"
    "v_mfma_f32_16x16x32_bf16 v[192:195], a[104:107], a[40:43], v[192:195]\n"
    "v_exp_f32 v201, v201\n"
    "v_cvt_pk_bf16_f32 v1, v2, v3\n"
    "v_mfma_f32_16x16x32_bf16 v[192:195], a[108:111], a[44:47], v[192:195]\n"
    "v_add_f32 v217, v217, v201\n"
    "v_mfma_f32_16x16x32_bf16 v[196:199], a[112:115], a[32:35], 0\n"
    "v_exp_f32 v202, v202\n"
    "v_cvt_pk_bf16_f32 v2, v4, v5\n"
    "v_mfma_f32_16x16x32_bf16 v[196:199], a[116:119], a[36:39], v[196:199]\n"
    "v_cvt_pk_bf16_f32 v212, v200, v201\n"
    "v_add_f32 v217, v217, v202\n"
    "v_cvt_pk_bf16_f32 v3, v6, v7\n"
    "v_mfma_f32_16x16x32_bf16 v[196:199], a[120:123], a[40:43], v[196:199]\n"
    "v_exp_f32 v203, v203\n"
    "v_mfma_f32_16x16x32_bf16 v[196:199], a[124:127], a[44:47], v[196:199]\n"
    "v_add_f32 v217, v217, v203\n"
    "ds_write_b128 v247, v[0:3] offset:0\n"
    "v_mfma_f32_16x16x32_bf16 v[32:35], a[160:163], v[208:211], v[32:35]\n"
    "v_exp_f32 v204, v204\n"
    "v_cvt_pk_bf16_f32 v8, v8, v9\n"
    "v_mfma_f32_16x16x32_bf16 v[36:39], a[164:167], v[208:211], v[36:39]\n"
    "v_cvt_pk_bf16_f32 v213, v202, v203\n"
    "v_add_f32 v217, v217, v204\n"
    "v_mfma_f32_16x16x32_bf16 v[40:43], a[168:171], v[208:211], v[40:43]\n"
    "v_exp_f32 v205, v205\n"
    "v_cvt_pk_bf16_f32 v9, v10, v11\n"
    "v_mfma_f32_16x16x32_bf16 v[44:47], a[172:175], v[208:211], v[44:47]\n"
    "v_add_f32 v217, v217, v205\n"
    "v_cvt_pk_bf16_f32 v10, v12, v13\n"
    "v_mfma_f32_16x16x32_bf16 v[48:51], a[176:179], v[208:211], v[48:51]\n"
    "v_exp_f32 v206, v206\n"
    "v_mfma_f32_16x16x32_bf16 v[52:55], a[180:183], v[208:211], v[52:55]\n"
    "v_cvt_pk_bf16_f32 v214, v204, v205\n"
    "v_add_f32 v217, v217, v206\n"
    "v_cvt_pk_bf16_f32 v11, v14, v15\n"
    "v_mfma_f32_16x16x32_bf16 v[56:59], a[184:187], v[208:211], v[56:59]\n"
    "v_exp_f32 v207, v207\n"
    "ds_write_b128 v247, v[8:11] offset:64\n"
    "v_mfma_f32_16x16x32_bf16 v[60:63], a[188:191], v[208:211], v[60:63]\n"
    "v_add_f32 v217, v217, v207\n"
    "v_mfma_f32_16x16x32_bf16 v[200:203], a[128:131], a[32:35], 0\n"
    "v_exp_f32 v192, v192\n"
    "v_mov_b32 v235, v217\n"
    "s_nop 1\n"
    "v_permlane16_swap_b32 v217, v235\n"
    "v_mfma_f32_16x16x32_bf16 v[200:203], a[132:135], a[36:39], v[200:203]\n"
    "v_cvt_pk_bf16_f32 v215, v206, v207\n"
    "v_add_f32 v218, v218, v192\n"
    "v_add_f32 v217, v217, v235\n"
    "v_mfma_f32_16x16x32_bf16 v[200:203], a[136:139], a[40:43], v[200:203]\n"
    "v_exp_f32 v193, v193\n"
    "v_mov_b32 v235, v217\n"
    "v_mfma_f32_16x16x32_bf16 v[200:203], a[140:143], a[44:47], v[200:203]\n"
    "v_add_f32 v218, v218, v193\n"
    "v_permlane32_swap_b32 v217, v235\n"
    "v_mfma_f32_16x16x32_bf16 v[204:207], a[144:147], a[32:35], 0\n"
    "v_exp_f32 v194, v194\n"
    "v_add_f32 v217, v217, v235\n"
    "v_mfma_f32_16x16x32_bf16 v[204:207], a[148:151], a[36:39], v[204:207]\n"
    "v_cvt_pk_bf16_f32 v208, v192, v193\n"
    "v_add_f32 v218, v218, v194\n"
    "v_rcp_f32 v232, v217\n"
    "v_mfma_f32_16x16x32_bf16 v[204:207], a[152:155], a[40:43], v[204:207]\n"
    "v_exp_f32 v195, v195\n"
    "v_max3_f32 v240, v240, v217, v232\n"
    "v_mfma_f32_16x16x32_bf16 v[204:207], a[156:159], a[44:47], v[204:207]\n"
    "v_add_f32 v218, v218, v195\n"
    "v_cvt_pk_bf16_f32 v16, v16, v17\n"
    "v_mfma_f32_16x16x32_bf16 v[32:35], a[192:195], v[212:215], v[32:35]\n"
    "v_exp_f32 v196, v196\n"
    "v_cvt_pk_bf16_f32 v17, v18, v19\n"
    "v_cvt_pk_bf16_f32 v18, v20, v21\n"
    "v_mfma_f32_16x16x32_bf16 v[36:39], a[196:199], v[212:215], v[36:39]\n"
    "v_cvt_pk_bf16_f32 v209, v194, v195\n"
    "v_add_f32 v218, v218, v196\n"
    "v_cvt_pk_bf16_f32 v19, v22, v23\n"
    "v_mfma_f32_16x16x32_bf16 v[40:43], a[200:203], v[212:215], v[40:43]\n"
    "v_exp_f32 v197, v197\n"
    "ds_write_b128 v247, v[16:19] offset:128\n"
    "v_mfma_f32_16x16x32_bf16 v[44:47], a[204:207], v[212:215], v[44:47]\n"
    "v_add_f32 v218, v218, v197\n"
    "v_cvt_pk_bf16_f32 v24, v24, v25\n"
    "v_mfma_f32_16x16x32_bf16 v[48:51], a[208:211], v[212:215], v[48:51]\n"
    "v_exp_f32 v198, v198\n"
    "v_cvt_pk_bf16_f32 v25, v26, v27\n"
    "v_mfma_f32_16x16x32_bf16 v[52:55], a[212:215], v[212:215], v[52:55]\n"
    "v_cvt_pk_bf16_f32 v210, v196, v197\n"
    "v_add_f32 v218, v218, v198\n"
    "v_cvt_pk_bf16_f32 v26, v28, v29\n"
    "v_mfma_f32_16x16x32_bf16 v[56:59], a[216:219], v[212:215], v[56:59]\n"
    "v_exp_f32 v199, v199\n"
    "v_cvt_pk_bf16_f32 v27, v30, v31\n"
    "v_mfma_f32_16x16x32_bf16 v[60:63], a[220:223], v[212:215], v[60:63]\n"
    "v_add_f32 v218, v218, v199\n"
    "ds_write_b128 v247, v[24:27] offset:192\n"
    "DF2%=:\n"
    "v_mfma_f32_16x16x32_bf16 v[192:195], a[96:99], a[48:51], 0\n"
    "v_exp_f32 v200, v200\n"
    "v_pk_mul_f32 v[32:33], v[32:33], v[232:233] op_sel_hi:[1,0]\n"
    "v_pk_mul_f32 v[34:35], v[34:35], v[232:233] op_sel_hi:[1,0]\n"
    "v_pk_mul_f32 v[36:37], v[36:37], v[232:233] op_sel_hi:[1,0]\n"
    "v_pk_mul_f32 v[38:39], v[38:39], v[232:233] op_sel_hi:[1,0]\n"
    "v_pk_mul_f32 v[40:41], v[40:41], v[232:233] op_sel_hi:[1,0]\n"
    "v_pk_mul_f32 v[42:43], v[42:43], v[232:233] op_sel_hi:[1,0]\n"
    "v_pk_mul_f32 v[44:45], v[44:45], v[232:233] op_sel_hi:[1,0]\n"
    "v_pk_mul_f32 v[46:47], v[46:47], v[232:233] op_sel_hi:[1,0]\n"
    "v_pk_mul_f32 v[48:49], v[48:49], v[232:233] op_sel_hi:[1,0]\n"
    "v_pk_mul_f32 v[50:51], v[50:51], v[232:233] op_sel_hi:[1,0]\n"
    "v_pk_mul_f32 v[52:53], v[52:53], v[232:233] op_sel_hi:[1,0]\n"
    "v_pk_mul_f32 v[54:55], v[54:55], v[232:233] op_sel_hi:[1,0]\n"
    "v_pk_mul_f32 v[56:57], v[56:57], v[232:233] op_sel_hi:[1,0]\n"
    "v_pk_mul_f32 v[58:59], v[58:59], v[232:233] op_sel_hi:[1,0]\n"
    "v_pk_mul_f32 v[60:61], v[60:61], v[232:233] op_sel_hi:[1,0]\n"
    "v_pk_mul_f32 v[62:63], v[62:63], v[232:233] op_sel_hi:[1,0]\n"
    "v_cvt_pk_bf16_f32 v32, v32, v33\n"
    "v_mfma_f32_16x16x32_bf16 v[192:195], a[100:103], a[52:55], v[192:195]\n"
    "v_cvt_pk_bf16_f32 v211, v198, v199\n"
    "v_add_f32 v218, v218, v200\n"
    "v_cvt_pk_bf16_f32 v33, v34, v35\n"
    "v_mfma_f32_16x16x32_bf16 v[192:195], a[104:107], a[56:59], v[192:195]\n"
    "v_exp_f32 v201, v201\n"
    "v_cvt_pk_bf16_f32 v34, v36, v37\n"
    "v_mfma_f32_16x16x32_bf16 v[192:195], a[108:111], a[60:63], v[192:195]\n"
    "v_add_f32 v218, v218, v201\n"
    "v_cvt_pk_bf16_f32 v35, v38, v39\n"
    "v_mfma_f32_16x16x32_bf16 v[196:199], a[112:115], a[48:51], 0\n"
    "v_exp_f32 v202, v202\n"
    "ds_write_b128 v247, v[32:35] offset:4352\n"
    "v_mfma_f32_16x16x32_bf16 v[196:199], a[116:119], a[52:55], v[196:199]\n"
    "v_cvt_pk_bf16_f32 v212, v200, v201\n"
    "v_add_f32 v218, v218, v202\n"
    "s_waitcnt lgkmcnt(1)\n"
    "v_mfma_f32_16x16x32_bf16 v[196:199], a[120:123], a[56:59], v[196:199]\n"
    "v_exp_f32 v203, v203\n"
    "v_add_u32 v249, 0, v248\n"
    "v_mfma_f32_16x16x32_bf16 v[196:199], a[124:127], a[60:63], v[196:199]\n"
    "v_add_f32 v218, v218, v203\n"
    "v_add_u32 v250, 2176, v248\n"
    "v_mfma_f32_16x16x32_bf16 v[64:67], a[160:163], v[208:211], v[64:67]\n"
    "v_exp_f32 v204, v204\n"
    "ds_read2_b64 v[0:3], v249 offset0:0 offset1:2\n"
    "ds_read2_b64 v[4:7], v249 offset0:136 offset1:138\n"
    "v_mfma_f32_16x16x32_bf16 v[68:71], a[164:167], v[208:211], v[68:71]\n"
    "v_cvt_pk_bf16_f32 v213, v202, v203\n"
    "v_add_f32 v218, v218, v204\n"
    "ds_read2_b64 v[8:11], v250 offset0:0 offset1:2\n"
    "v_mfma_f32_16x16x32_bf16 v[72:75], a[168:171], v[208:211], v[72:75]\n"
    "v_exp_f32 v205, v205\n"
    "ds_read2_b64 v[12:15], v250 offset0:136 offset1:138\n"
    "v_mfma_f32_16x16x32_bf16 v[76:79], a[172:175], v[208:211], v[76:79]\n"
    "v_add_f32 v218, v218, v205\n"
    "v_cvt_pk_bf16_f32 v40, v40, v41\n"
    "v_mfma_f32_16x16x32_bf16 v[80:83], a[176:179], v[208:211], v[80:83]\n"
    "v_exp_f32 v206, v206\n"
    "v_cvt_pk_bf16_f32 v41, v42, v43\n"
    "v_mfma_f32_16x16x32_bf16 v[84:87], a[180:183], v[208:211], v[84:87]\n"
    "v_cvt_pk_bf16_f32 v214, v204, v205\n"
    "v_add_f32 v218, v218, v206\n"
    "v_cvt_pk_bf16_f32 v42, v44, v45\n"
    "v_mfma_f32_16x16x32_bf16 v[88:91], a[184:187], v[208:211], v[88:91]\n"
    "v_exp_f32 v207, v207\n"
    "v_cvt_pk_bf16_f32 v43, v46, v47\n"
    "v_mfma_f32_16x16x32_bf16 v[92:95], a[188:191], v[208:211], v[92:95]\n"
    "v_add_f32 v218, v218, v207\n"
    "ds_write_b128 v247, v[40:43] offset:4416\n"
    "v_mfma_f32_16x16x32_bf16 v[200:203], a[128:131], a[48:51], 0\n"
    "v_exp_f32 v192, v192\n"
    "v_mov_b32 v235, v218\n"
    "s_nop 1\n"
    "v_permlane16_swap_b32 v218, v235\n"
    "v_mfma_f32_16x16x32_bf16 v[200:203], a[132:135], a[52:55], v[200:203]\n"
    "v_cvt_pk_bf16_f32 v215, v206, v207\n"
    "v_add_f32 v219, v219, v192\n"
    "v_add_f32 v218, v218, v235\n"
    "v_mfma_f32_16x16x32_bf16 v[200:203], a[136:139], a[56:59], v[200:203]\n"
    "v_exp_f32 v193, v193\n"
    "v_mov_b32 v235, v218\n"
    "v_mfma_f32_16x16x32_bf16 v[200:203], a[140:143], a[60:63], v[200:203]\n"
    "v_add_f32 v219, v219, v193\n"
    "v_permlane32_swap_b32 v218, v235\n"
    "v_mfma_f32_16x16x32_bf16 v[204:207], a[144:147], a[48:51], 0\n"
    "v_exp_f32 v194, v194\n"
    "v_add_f32 v218, v218, v235\n"
    "v_rcp_f32 v230, v218\n"
    "v_mfma_f32_16x16x32_bf16 v[204:207], a[148:151], a[52:55], v[204:207]\n"
    "v_cvt_pk_bf16_f32 v208, v192, v193\n"
    "v_add_f32 v219, v219, v194\n"
    "v_max3_f32 v240, v240, v218, v230\n"
    "v_mfma_f32_16x16x32_bf16 v[204:207], a[152:155], a[56:59], v[204:207]\n"
    "v_exp_f32 v195, v195\n"
    "v_cvt_pk_bf16_f32 v48, v48, v49\n"
    "v_mfma_f32_16x16x32_bf16 v[204:207], a[156:159], a[60:63], v[204:207]\n"
    "v_add_f32 v219, v219, v195\n"
    "v_cvt_pk_bf16_f32 v49, v50, v51\n"
    "v_mfma_f32_16x16x32_bf16 v[64:67], a[192:195], v[212:215], v[64:67]\n"
    "v_exp_f32 v196, v196\n"
    "v_cvt_pk_bf16_f32 v50, v52, v53\n"
    "v_cvt_pk_bf16_f32 v51, v54, v55\n"
    "v_mfma_f32_16x16x32_bf16 v[68:71], a[196:199], v[212:215], v[68:71]\n"
    "v_cvt_pk_bf16_f32 v209, v194, v195\n"
    "v_add_f32 v219, v219, v196\n"
    "ds_write_b128 v247, v[48:51] offset:4480\n"
    "v_mfma_f32_16x16x32_bf16 v[72:75], a[200:203], v[212:215], v[72:75]\n"
    "v_exp_f32 v197, v197\n"
    "s_waitcnt lgkmcnt(2)\n"
    "v_mfma_f32_16x16x32_bf16 v[76:79], a[204:207], v[212:215], v[76:79]\n"
    "v_add_f32 v219, v219, v197\n"
    "s_add_u32 s43, s42, 0\n"
    "buffer_store_dwordx4 v[0:3], v229, s[96:99], s43 offen sc0 sc1\n"
    "buffer_store_dwordx4 v[4:7], v229, s[96:99], s43 offen offset:1024 sc0 sc1\n"
    "buffer_store_dwordx4 v[8:11], v229, s[96:99], s43 offen offset:2048 sc0 sc1\n"
    "buffer_store_dwordx4 v[12:15], v229, s[96:99], s43 offen offset:3072 sc0 sc1\n"
    "v_mfma_f32_16x16x32_bf16 v[80:83], a[208:211], v[212:215], v[80:83]\n"
    "v_exp_f32 v198, v198\n"
    "v_cvt_pk_bf16_f32 v56, v56, v57\n"
    "v_cvt_pk_bf16_f32 v57, v58, v59\n"
    "v_mfma_f32_16x16x32_bf16 v[84:87], a[212:215], v[212:215], v[84:87]\n"
    "v_cvt_pk_bf16_f32 v210, v196, v197\n"
    "v_add_f32 v219, v219, v198\n"
    "v_cvt_pk_bf16_f32 v58, v60, v61\n"
    "v_mfma_f32_16x16x32_bf16 v[88:91], a[216:219], v[212:215], v[88:91]\n"
    "v_exp_f32 v199, v199\n"
    "v_cvt_pk_bf16_f32 v59, v62, v63\n"
    "v_mfma_f32_16x16x32_bf16 v[92:95], a[220:223], v[212:215], v[92:95]\n"
    "v_add_f32 v219, v219, v199\n"
    "ds_write_b128 v247, v[56:59] offset:4544\n"
    "DF3%=:\n"
    "v_mfma_f32_16x16x32_bf16 v[192:195], a[96:99], a[64:67], 0\n"
    "v_exp_f32 v200, v200\n"
    "v_pk_mul_f32 v[64:65], v[64:65], v[230:231] op_sel_hi:[1,0]\n"
    "v_pk_mul_f32 v[66:67], v[66:67], v[230:231] op_sel_hi:[1,0]\n"
    "v_pk_mul_f32 v[68:69], v[68:69], v[230:231] op_sel_hi:[1,0]\n"
    "v_pk_mul_f32 v[70:71], v[70:71], v[230:231] op_sel_hi:[1,0]\n"
    "v_pk_mul_f32 v[72:73], v[72:73], v[230:231] op_sel_hi:[1,0]\n"
    "v_pk_mul_f32 v[74:75], v[74:75], v[230:231] op_sel_hi:[1,0]\n"
    "v_pk_mul_f32 v[76:77], v[76:77], v[230:231] op_sel_hi:[1,0]\n"
    "v_pk_mul_f32 v[78:79], v[78:79], v[230:231] op_sel_hi:[1,0]\n"
    "v_pk_mul_f32 v[80:81], v[80:81], v[230:231] op_sel_hi:[1,0]\n"
    "v_pk_mul_f32 v[82:83], v[82:83], v[230:231] op_sel_hi:[1,0]\n"
    "v_pk_mul_f32 v[84:85], v[84:85], v[230:231] op_sel_hi:[1,0]\n"
    "v_pk_mul_f32 v[86:87], v[86:87], v[230:231] op_sel_hi:[1,0]\n"
    "v_pk_mul_f32 v[88:89], v[88:89], v[230:231] op_sel_hi:[1,0]\n"
    "v_pk_mul_f32 v[90:91], v[90:91], v[230:231] op_sel_hi:[1,0]\n"
    "v_pk_mul_f32 v[92:93], v[92:93], v[230:231] op_sel_hi:[1,0]\n"
    "v_pk_mul_f32 v[94:95], v[94:95], v[230:231] op_sel_hi:[1,0]\n"
    "v_cvt_pk_bf16_f32 v64, v64, v65\n"
    "v_mfma_f32_16x16x32_bf16 v[192:195], a[100:103], a[68:71], v[192:195]\n"
    "v_cvt_pk_bf16_f32 v211, v198, v199\n"
    "v_add_f32 v219, v219, v200\n"
    "v_cvt_pk_bf16_f32 v65, v66, v67\n"
    "v_mfma_f32_16x16x32_bf16 v[192:195], a[104:107], a[72:75], v[192:195]\n"
    "v_exp_f32 v201, v201\n"
    "v_cvt_pk_bf16_f32 v66, v68, v69\n"
    "v_mfma_f32_16x16x32_bf16 v[192:195], a[108:111], a[76:79], v[192:195]\n"
    "v_add_f32 v219, v219, v201\n"
    "v_cvt_pk_bf16_f32 v67, v70, v71\n"
    "v_mfma_f32_16x16x32_bf16 v[196:199], a[112:115], a[64:67], 0\n"
    "v_exp_f32 v202, v202\n"
    "ds_write_b128 v247, v[64:67] offset:8704\n"
    "v_mfma_f32_16x16x32_bf16 v[196:199], a[116:119], a[68:71], v[196:199]\n"
    "v_cvt_pk_bf16_f32 v212, v200, v201\n"
    "v_add_f32 v219, v219, v202\n"
    "s_waitcnt lgkmcnt(1)\n"
    "v_mfma_f32_16x16x32_bf16 v[196:199], a[120:123], a[72:75], v[196:199]\n"
    "v_exp_f32 v203, v203\n"
    "v_add_u32 v249, 4352, v248\n"
    "v_mfma_f32_16x16x32_bf16 v[196:199], a[124:127], a[76:79], v[196:199]\n"
    "v_add_f32 v219, v219, v203\n"
    "v_add_u32 v250, 6528, v248\n"
    "v_mfma_f32_16x16x32_bf16 v[96:99], a[160:163], v[208:211], v[96:99]\n"
    "v_exp_f32 v204, v204\n"
    "ds_read2_b64 v[32:35], v249 offset0:0 offset1:2\n"
    "ds_read2_b64 v[36:39], v249 offset0:136 offset1:138\n"
    "v_mfma_f32_16x16x32_bf16 v[100:103], a[164:167], v[208:211], v[100:103]\n"
    "v_cvt_pk_bf16_f32 v213, v202, v203\n"
    "v_add_f32 v219, v219, v204\n"
    "ds_read2_b64 v[40:43], v250 offset0:0 offset1:2\n"
    "v_mfma_f32_16x16x32_bf16 v[104:107], a[168:171], v[208:211], v[104:107]\n"
    "v_exp_f32 v205, v205\n"
    "ds_read2_b64 v[44:47], v250 offset0:136 offset1:138\n"
    "v_mfma_f32_16x16x32_bf16 v[108:111], a[172:175], v[208:211], v[108:111]\n"
    "v_add_f32 v219, v219, v205\n"
    "v_cvt_pk_bf16_f32 v72, v72, v73\n"
    "v_mfma_f32_16x16x32_bf16 v[112:115], a[176:179], v[208:211], v[112:115]\n"
    "v_exp_f32 v206, v206\n"
    "v_cvt_pk_bf16_f32 v73, v74, v75\n"
    "v_mfma_f32_16x16x32_bf16 v[116:119], a[180:183], v[208:211], v[116:119]\n"
    "v_cvt_pk_bf16_f32 v214, v204, v205\n"
    "v_add_f32 v219, v219, v206\n"
    "v_cvt_pk_bf16_f32 v74, v76, v77\n"
    "v_mfma_f32_16x16x32_bf16 v[120:123], a[184:187], v[208:211], v[120:123]\n"
    "v_exp_f32 v207, v207\n"
    "v_cvt_pk_bf16_f32 v75, v78, v79\n"
    "v_mfma_f32_16x16x32_bf16 v[124:127], a[188:191], v[208:211], v[124:127]\n"
    "v_add_f32 v219, v219, v207\n"
    "ds_write_b128 v247, v[72:75] offset:8768\n"
    "v_mfma_f32_16x16x32_bf16 v[200:203], a[128:131], a[64:67], 0\n"
    "v_exp_f32 v192, v192\n"
    "v_mov_b32 v235, v219\n"
    "s_nop 1\n"
    "v_permlane16_swap_b32 v219, v235\n"
    "v_mfma_f32_16x16x32_bf16 v[200:203], a[132:135], a[68:71], v[200:203]\n"
    "v_cvt_pk_bf16_f32 v215, v206, v207\n"
    "v_add_f32 v220, v220, v192\n"
    "v_add_f32 v219, v219, v235\n"
    "v_mfma_f32_16x16x32_bf16 v[200:203], a[136:139], a[72:75], v[200:203]\n"
    "v_exp_f32 v193, v193\n"
    "v_mov_b32 v235, v219\n"
    "v_mfma_f32_16x16x32_bf16 v[200:203], a[140:143], a[76:79], v[200:203]\n"
    "v_add_f32 v220, v220, v193\n"
    "v_permlane32_swap_b32 v219, v235\n"
    "v_mfma_f32_16x16x32_bf16 v[204:207], a[144:147], a[64:67], 0\n"
    "v_exp_f32 v194, v194\n"
    "v_add_f32 v219, v219, v235\n"
    "v_rcp_f32 v232, v219\n"
    "v_mfma_f32_16x16x32_bf16 v[204:207], a[148:151], a[68:71], v[204:207]\n"
    "v_cvt_pk_bf16_f32 v208, v192, v193\n"
    "v_add_f32 v220, v220, v194\n"
    "v_max3_f32 v240, v240, v219, v232\n"
    "v_mfma_f32_16x16x32_bf16 v[204:207], a[152:155], a[72:75], v[204:207]\n"
    "v_exp_f32 v195, v195\n"
    "v_cvt_pk_bf16_f32 v80, v80, v81\n"
    "v_mfma_f32_16x16x32_bf16 v[204:207], a[156:159], a[76:79], v[204:207]\n"
    "v_add_f32 v220, v220, v195\n"
    "v_cvt_pk_bf16_f32 v81, v82, v83\n"
    "v_mfma_f32_16x16x32_bf16 v[96:99], a[192:195], v[212:215], v[96:99]\n"
    "v_exp_f32 v196, v196\n"
    "v_cvt_pk_bf16_f32 v82, v84, v85\n"
    "v_cvt_pk_bf16_f32 v83, v86, v87\n"
    "v_mfma_f32_16x16x32_bf16 v[100:103], a[196:199], v[212:215], v[100:103]\n"
    "v_cvt_pk_bf16_f32 v209, v194, v195\n"
    "v_add_f32 v220, v220, v196\n"
    "ds_write_b128 v247, v[80:83] offset:8832\n"
    "v_mfma_f32_16x16x32_bf16 v[104:107], a[200:203], v[212:215], v[104:107]\n"
    "v_exp_f32 v197, v197\n"
    "s_waitcnt lgkmcnt(2)\n"
    "v_mfma_f32_16x16x32_bf16 v[108:111], a[204:207], v[212:215], v[108:111]\n"
    "v_add_f32 v220, v220, v197\n"
    "s_add_u32 s43, s42, 4096\n"
    "buffer_store_dwordx4 v[32:35], v229, s[96:99], s43 offen sc0 sc1\n"
    "buffer_store_dwordx4 v[36:39], v229, s[96:99], s43 offen offset:1024 sc0 sc1\n"
    "buffer_store_dwordx4 v[40:43], v229, s[96:99], s43 offen offset:2048 sc0 sc1\n"
    "buffer_store_dwordx4 v[44:47], v229, s[96:99], s43 offen offset:3072 sc0 sc1\n"
    "v_mfma_f32_16x16x32_bf16 v[112:115], a[208:211], v[212:215], v[112:115]\n"
    "v_exp_f32 v198, v198\n"
    "v_cvt_pk_bf16_f32 v88, v88, v89\n"
    "v_cvt_pk_bf16_f32 v89, v90, v91\n"
    "v_mfma_f32_16x16x32_bf16 v[116:119], a[212:215], v[212:215], v[116:119]\n"
    "v_cvt_pk_bf16_f32 v210, v196, v197\n"
    "v_add_f32 v220, v220, v198\n"
    "v_cvt_pk_bf16_f32 v90, v92, v93\n"
    "v_mfma_f32_16x16x32_bf16 v[120:123], a[216:219], v[212:215], v[120:123]\n"
    "v_exp_f32 v199, v199\n"
    "v_cvt_pk_bf16_f32 v91, v94, v95\n"
    "v_mfma_f32_16x16x32_bf16 v[124:127], a[220:223], v[212:215], v[124:127]\n"
    "v_add_f32 v220, v220, v199\n"
    "ds_write_b128 v247, v[88:91] offset:8896\n"
    "DF4%=:\n"
    "v_mfma_f32_16x16x32_bf16 v[192:195], a[96:99], a[80:83], 0\n"
    "v_exp_f32 v200, v200\n"
    "v_pk_mul_f32 v[96:97], v[96:97], v[232:233] op_sel_hi:[1,0]\n"
    "v_pk_mul_f32 v[98:99], v[98:99], v[232:233] op_sel_hi:[1,0]\n"
    "v_pk_mul_f32 v[100:101], v[100:101], v[232:233] op_sel_hi:[1,0]\n"
    "v_pk_mul_f32 v[102:103], v[102:103], v[232:233] op_sel_hi:[1,0]\n"
    "v_pk_mul_f32 v[104:105], v[104:105], v[232:233] op_sel_hi:[1,0]\n"
    "v_pk_mul_f32 v[106:107], v[106:107], v[232:233] op_sel_hi:[1,0]\n"
    "v_pk_mul_f32 v[108:109], v[108:109], v[232:233] op_sel_hi:[1,0]\n"
    "v_pk_mul_f32 v[110:111], v[110:111], v[232:233] op_sel_hi:[1,0]\n"
    "v_pk_mul_f32 v[112:113], v[112:113], v[232:233] op_sel_hi:[1,0]\n"
    "v_pk_mul_f32 v[114:115], v[114:115], v[232:233] op_sel_hi:[1,0]\n"
    "v_pk_mul_f32 v[116:117], v[116:117], v[232:233] op_sel_hi:[1,0]\n"
    "v_pk_mul_f32 v[118:119], v[118:119], v[232:233] op_sel_hi:[1,0]\n"
    "v_pk_mul_f32 v[120:121], v[120:121], v[232:233] op_sel_hi:[1,0]\n"
    "v_pk_mul_f32 v[122:123], v[122:123], v[232:233] op_sel_hi:[1,0]\n"
    "v_pk_mul_f32 v[124:125], v[124:125], v[232:233] op_sel_hi:[1,0]\n"
    "v_pk_mul_f32 v[126:127], v[126:127], v[232:233] op_sel_hi:[1,0]\n"
    "v_cvt_pk_bf16_f32 v96, v96, v97\n"
    "v_mfma_f32_16x16x32_bf16 v[192:195], a[100:103], a[84:87], v[192:195]\n"
    "v_cvt_pk_bf16_f32 v211, v198, v199\n"
    "v_add_f32 v220, v220, v200\n"
    "v_cvt_pk_bf16_f32 v97, v98, v99\n"
    "v_mfma_f32_16x16x32_bf16 v[192:195], a[104:107], a[88:91], v[192:195]\n"
    "v_exp_f32 v201, v201\n"
    "v_cvt_pk_bf16_f32 v98, v100, v101\n"
    "v_mfma_f32_16x16x32_bf16 v[192:195], a[108:111], a[92:95], v[192:195]\n"
    "v_add_f32 v220, v220, v201\n"
    "v_cvt_pk_bf16_f32 v99, v102, v103\n"
    "v_mfma_f32_16x16x32_bf16 v[196:199], a[112:115], a[80:83], 0\n"
    "v_exp_f32 v202, v202\n"
    "ds_write_b128 v247, v[96:99] offset:13056\n"
    "v_mfma_f32_16x16x32_bf16 v[196:199], a[116:119], a[84:87], v[196:199]\n"
    "v_cvt_pk_bf16_f32 v212, v200, v201\n"
    "v_add_f32 v220, v220, v202\n"
    "s_waitcnt lgkmcnt(1)\n"
    "v_mfma_f32_16x16x32_bf16 v[196:199], a[120:123], a[88:91], v[196:199]\n"
    "v_exp_f32 v203, v203\n"
    "v_add_u32 v249, 8704, v248\n"
    "v_mfma_f32_16x16x32_bf16 v[196:199], a[124:127], a[92:95], v[196:199]\n"
    "v_add_f32 v220, v220, v203\n"
    "v_add_u32 v250, 10880, v248\n"
    "v_mfma_f32_16x16x32_bf16 v[128:131], a[160:163], v[208:211], v[128:131]\n"
    "v_exp_f32 v204, v204\n"
    "ds_read2_b64 v[64:67], v249 offset0:0 offset1:2\n"
    "ds_read2_b64 v[68:71], v249 offset0:136 offset1:138\n"
    "v_mfma_f32_16x16x32_bf16 v[132:135], a[164:167], v[208:211], v[132:135]\n"
    "v_cvt_pk_bf16_f32 v213, v202, v203\n"
    "v_add_f32 v220, v220, v204\n"
    "ds_read2_b64 v[72:75], v250 offset0:0 offset1:2\n"
    "v_mfma_f32_16x16x32_bf16 v[136:139], a[168:171], v[208:211], v[136:139]\n"
    "v_exp_f32 v205, v205\n"
    "ds_read2_b64 v[76:79], v250 offset0:136 offset1:138\n"
    "v_mfma_f32_16x16x32_bf16 v[140:143], a[172:175], v[208:211], v[140:143]\n"
    "v_add_f32 v220, v220, v205\n"
    "v_cvt_pk_bf16_f32 v104, v104, v105\n"
    "v_mfma_f32_16x16x32_bf16 v[144:147], a[176:179], v[208:211], v[144:147]\n"
    "v_exp_f32 v206, v206\n"
    "v_cvt_pk_bf16_f32 v105, v106, v107\n"
    "v_mfma_f32_16x16x32_bf16 v[148:151], a[180:183], v[208:211], v[148:151]\n"
    "v_cvt_pk_bf16_f32 v214, v204, v205\n"
    "v_add_f32 v220, v220, v206\n"
    "v_cvt_pk_bf16_f32 v106, v108, v109\n"
    "v_mfma_f32_16x16x32_bf16 v[152:155], a[184:187], v[208:211], v[152:155]\n"
    "v_exp_f32 v207, v207\n"
    "v_cvt_pk_bf16_f32 v107, v110, v111\n"
    "v_mfma_f32_16x16x32_bf16 v[156:159], a[188:191], v[208:211], v[156:159]\n"
    "v_add_f32 v220, v220, v207\n"
    "ds_write_b128 v247, v[104:107] offset:13120\n"
    "v_mfma_f32_16x16x32_bf16 v[200:203], a[128:131], a[80:83], 0\n"
    "v_exp_f32 v192, v192\n"
    "v_mov_b32 v235, v220\n"
    "s_nop 1\n"
    "v_permlane16_swap_b32 v220, v235\n"
    "v_mfma_f32_16x16x32_bf16 v[200:203], a[132:135], a[84:87], v[200:203]\n"
    "v_cvt_pk_bf16_f32 v215, v206, v207\n"
    "v_add_f32 v221, v221, v192\n"
    "v_add_f32 v220, v220, v235\n"
    "v_mfma_f32_16x16x32_bf16 v[200:203], a[136:139], a[88:91], v[200:203]\n"
    "v_exp_f32 v193, v193\n"
    "v_mov_b32 v235, v220\n"
    "v_mfma_f32_16x16x32_bf16 v[200:203], a[140:143], a[92:95], v[200:203]\n"
    "v_add_f32 v221, v221, v193\n"
    "v_permlane32_swap_b32 v220, v235\n"
    "v_mfma_f32_16x16x32_bf16 v[204:207], a[144:147], a[80:83], 0\n"
    "v_exp_f32 v194, v194\n"
    "v_add_f32 v220, v220, v235\n"
    "v_rcp_f32 v230, v220\n"
    "v_mfma_f32_16x16x32_bf16 v[204:207], a[148:151], a[84:87], v[204:207]\n"
    "v_cvt_pk_bf16_f32 v208, v192, v193\n"
    "v_add_f32 v221, v221, v194\n"
    "v_max3_f32 v240, v240, v220, v230\n"
    "v_mfma_f32_16x16x32_bf16 v[204:207], a[152:155], a[88:91], v[204:207]\n"
    "v_exp_f32 v195, v195\n"
    "v_cvt_pk_bf16_f32 v112, v112, v113\n"
    "v_mfma_f32_16x16x32_bf16 v[204:207], a[156:159], a[92:95], v[204:207]\n"
    "v_add_f32 v221, v221, v195\n"
    "v_cvt_pk_bf16_f32 v113, v114, v115\n"
    "v_mfma_f32_16x16x32_bf16 v[128:131], a[192:195], v[212:215], v[128:131]\n"
    "v_exp_f32 v196, v196\n"
    "v_cvt_pk_bf16_f32 v114, v116, v117\n"
    "v_cvt_pk_bf16_f32 v115, v118, v119\n"
    "v_mfma_f32_16x16x32_bf16 v[132:135], a[196:199], v[212:215], v[132:135]\n"
    "v_cvt_pk_bf16_f32 v209, v194, v195\n"
    "v_add_f32 v221, v221, v196\n"
    "ds_write_b128 v247, v[112:115] offset:13184\n"
    "v_mfma_f32_16x16x32_bf16 v[136:139], a[200:203], v[212:215], v[136:139]\n"
    "v_exp_f32 v197, v197\n"
    "s_waitcnt lgkmcnt(2)\n"
    "v_mfma_f32_16x16x32_bf16 v[140:143], a[204:207], v[212:215], v[140:143]\n"
    "v_add_f32 v221, v221, v197\n"
    "s_add_u32 s43, s42, 8192\n"
    "buffer_store_dwordx4 v[64:67], v229, s[96:99], s43 offen sc0 sc1\n"
    "buffer_store_dwordx4 v[68:71], v229, s[96:99], s43 offen offset:1024 sc0 sc1\n"
    "buffer_store_dwordx4 v[72:75], v229, s[96:99], s43 offen offset:2048 sc0 sc1\n"
    "buffer_store_dwordx4 v[76:79], v229, s[96:99], s43 offen offset:3072 sc0 sc1\n"
    "v_mfma_f32_16x16x32_bf16 v[144:147], a[208:211], v[212:215], v[144:147]\n"
    "v_exp_f32 v198, v198\n"
    "v_cvt_pk_bf16_f32 v120, v120, v121\n"
    "v_cvt_pk_bf16_f32 v121, v122, v123\n"
    "v_mfma_f32_16x16x32_bf16 v[148:151], a[212:215], v[212:215], v[148:151]\n"
    "v_cvt_pk_bf16_f32 v210, v196, v197\n"
    "v_add_f32 v221, v221, v198\n"
    "v_cvt_pk_bf16_f32 v122, v124, v125\n"
    "v_mfma_f32_16x16x32_bf16 v[152:155], a[216:219], v[212:215], v[152:155]\n"
    "v_exp_f32 v199, v199\n"
    "v_cvt_pk_bf16_f32 v123, v126, v127\n"
    "v_mfma_f32_16x16x32_bf16 v[156:159], a[220:223], v[212:215], v[156:159]\n"
    "v_add_f32 v221, v221, v199\n"
    "ds_write_b128 v247, v[120:123] offset:13248\n"
    "s_cmp_eq_u32 s59, 3\n"
    "s_cbranch_scc1 ENDB%=\n"
    "DF5%=:\n"
    "v_cvt_pk_bf16_f32 v211, v198, v199\n"
    "s_nop 1\n"
    "v_mfma_f32_16x16x32_bf16 v[160:163], a[160:163], v[208:211], v[160:163]\n"
    "v_exp_f32 v200, v200\n"
    "v_pk_mul_f32 v[128:129], v[128:129], v[230:231] op_sel_hi:[1,0]\n"
    "v_pk_mul_f32 v[130:131], v[130:131], v[230:231] op_sel_hi:[1,0]\n"
    "v_pk_mul_f32 v[132:133], v[132:133], v[230:231] op_sel_hi:[1,0]\n"
    "v_pk_mul_f32 v[134:135], v[134:135], v[230:231] op_sel_hi:[1,0]\n"
    "v_pk_mul_f32 v[136:137], v[136:137], v[230:231] op_sel_hi:[1,0]\n"
    "v_pk_mul_f32 v[138:139], v[138:139], v[230:231] op_sel_hi:[1,0]\n"
    "v_pk_mul_f32 v[140:141], v[140:141], v[230:231] op_sel_hi:[1,0]\n"
    "v_pk_mul_f32 v[142:143], v[142:143], v[230:231] op_sel_hi:[1,0]\n"
    "v_pk_mul_f32 v[144:145], v[144:145], v[230:231] op_sel_hi:[1,0]\n"
    "v_pk_mul_f32 v[146:147], v[146:147], v[230:231] op_sel_hi:[1,0]\n"
    "v_pk_mul_f32 v[148:149], v[148:149], v[230:231] op_sel_hi:[1,0]\n"
    "v_pk_mul_f32 v[150:151], v[150:151], v[230:231] op_sel_hi:[1,0]\n"
    "v_pk_mul_f32 v[152:153], v[152:153], v[230:231] op_sel_hi:[1,0]\n"
    "v_pk_mul_f32 v[154:155], v[154:155], v[230:231] op_sel_hi:[1,0]\n"
    "v_pk_mul_f32 v[156:157], v[156:157], v[230:231] op_sel_hi:[1,0]\n"
    "v_pk_mul_f32 v[158:159], v[158:159], v[230:231] op_sel_hi:[1,0]\n"
    "v_exp_f32 v201, v201\n"
    "v_cvt_pk_bf16_f32 v128, v128, v129\n"
    "v_exp_f32 v202, v202\n"
    "v_mfma_f32_16x16x32_bf16 v[164:167], a[164:167], v[208:211], v[164:167]\n"
    "v_cvt_pk_bf16_f32 v129, v130, v131\n"
    "v_exp_f32 v203, v203\n"
    "v_cvt_pk_bf16_f32 v130, v132, v133\n"
    "v_cvt_pk_bf16_f32 v212, v200, v201\n"
    "v_cvt_pk_bf16_f32 v131, v134, v135\n"
    "v_mfma_f32_16x16x32_bf16 v[168:171], a[168:171], v[208:211], v[168:171]\n"
    "v_add_f32 v221, v221, v200\n"
    "ds_write_b128 v247, v[128:131] offset:17408\n"
    "v_exp_f32 v204, v204\n"
    "s_waitcnt lgkmcnt(1)\n"
    "v_cvt_pk_bf16_f32 v213, v202, v203\n"
    "v_mfma_f32_16x16x32_bf16 v[172:175], a[172:175], v[208:211], v[172:175]\n"
    "v_add_u32 v249, 13056, v248\n"
    "v_add_f32 v221, v221, v201\n"
    "v_add_u32 v250, 15232, v248\n"
    "v_exp_f32 v205, v205\n"
    "ds_read2_b64 v[96:99], v249 offset0:0 offset1:2\n"
    "v_mfma_f32_16x16x32_bf16 v[176:179], a[176:179], v[208:211], v[176:179]\n"
    "v_add_f32 v221, v221, v202\n"
    "ds_read2_b64 v[100:103], v249 offset0:136 offset1:138\n"
    "v_exp_f32 v206, v206\n"
    "ds_read2_b64 v[104:107], v250 offset0:0 offset1:2\n"
    "v_add_f32 v221, v221, v203\n"
    "v_mfma_f32_16x16x32_bf16 v[180:183], a[180:183], v[208:211], v[180:183]\n"
    "ds_read2_b64 v[108:111], v250 offset0:136 offset1:138\n"
    "v_exp_f32 v207, v207\n"
    "v_cvt_pk_bf16_f32 v136, v136, v137\n"
    "v_add_f32 v221, v221, v204\n"
    "v_cvt_pk_bf16_f32 v137, v138, v139\n"
    "v_mfma_f32_16x16x32_bf16 v[184:187], a[184:187], v[208:211], v[184:187]\n"
    "v_cvt_pk_bf16_f32 v214, v204, v205\n"
    "v_cvt_pk_bf16_f32 v138, v140, v141\n"
    "v_add_f32 v221, v221, v205\n"
    "v_cvt_pk_bf16_f32 v139, v142, v143\n"
    "v_add_f32 v221, v221, v206\n"
    "v_mfma_f32_16x16x32_bf16 v[188:191], a[188:191], v[208:211], v[188:191]\n"
    "ds_write_b128 v247, v[136:139] offset:17472\n"
    "v_cvt_pk_bf16_f32 v215, v206, v207\n"
    "v_add_f32 v221, v221, v207\n"
    "s_nop 1\n"
    "v_mfma_f32_16x16x32_bf16 v[160:163], a[192:195], v[212:215], v[160:163]\n"
    "v_mov_b32 v235, v221\n"
    "s_nop 1\n"
    "v_permlane16_swap_b32 v221, v235\n"
    "v_add_f32 v221, v221, v235\n"
    "v_mfma_f32_16x16x32_bf16 v[164:167], a[196:199], v[212:215], v[164:167]\n"
    "v_mov_b32 v235, v221\n"
    "s_nop 1\n"
    "v_permlane32_swap_b32 v221, v235\n"
    "v_add_f32 v221, v221, v235\n"
    "v_mfma_f32_16x16x32_bf16 v[168:171], a[200:203], v[212:215], v[168:171]\n"
    "v_rcp_f32 v232, v221\n"
    "s_nop 0\n"
    "v_max3_f32 v240, v240, v221, v232\n"
    "v_cvt_pk_bf16_f32 v144, v144, v145\n"
    "v_mfma_f32_16x16x32_bf16 v[172:175], a[204:207], v[212:215], v[172:175]\n"
    "v_cvt_pk_bf16_f32 v145, v146, v147\n"
    "v_cvt_pk_bf16_f32 v146, v148, v149\n"
    "v_cvt_pk_bf16_f32 v147, v150, v151\n"
    "v_mfma_f32_16x16x32_bf16 v[176:179], a[208:211], v[212:215], v[176:179]\n"
    "ds_write_b128 v247, v[144:147] offset:17536\n"
    "s_waitcnt lgkmcnt(2)\n"
    "s_add_u32 s43, s42, 12288\n"
    "buffer_store_dwordx4 v[96:99], v229, s[96:99], s43 offen sc0 sc1\n"
    "buffer_store_dwordx4 v[100:103], v229, s[96:99], s43 offen offset:1024 sc0 sc1\n"
    "buffer_store_dwordx4 v[104:107], v229, s[96:99], s43 offen offset:2048 sc0 sc1\n"
    "buffer_store_dwordx4 v[108:111], v229, s[96:99], s43 offen offset:3072 sc0 sc1\n"
    "v_mfma_f32_16x16x32_bf16 v[180:183], a[212:215], v[212:215], v[180:183]\n"
    "v_cvt_pk_bf16_f32 v152, v152, v153\n"
    "v_cvt_pk_bf16_f32 v153, v154, v155\n"
    "v_cvt_pk_bf16_f32 v154, v156, v157\n"
    "v_mfma_f32_16x16x32_bf16 v[184:187], a[216:219], v[212:215], v[184:187]\n"
    "v_cvt_pk_bf16_f32 v155, v158, v159\n"
    "ds_write_b128 v247, v[152:155] offset:17600\n"
    "v_mfma_f32_16x16x32_bf16 v[188:191], a[220:223], v[212:215], v[188:191]\n"
    "v_pk_mul_f32 v[160:161], v[160:161], v[232:233] op_sel_hi:[1,0]\n"
    "v_pk_mul_f32 v[162:163], v[162:163], v[232:233] op_sel_hi:[1,0]\n"
    "v_pk_mul_f32 v[164:165], v[164:165], v[232:233] op_sel_hi:[1,0]\n"
    "v_pk_mul_f32 v[166:167], v[166:167], v[232:233] op_sel_hi:[1,0]\n"
    "v_pk_mul_f32 v[168:169], v[168:169], v[232:233] op_sel_hi:[1,0]\n"
    "v_pk_mul_f32 v[170:171], v[170:171], v[232:233] op_sel_hi:[1,0]\n"
    "v_pk_mul_f32 v[172:173], v[172:173], v[232:233] op_sel_hi:[1,0]\n"
    "v_pk_mul_f32 v[174:175], v[174:175], v[232:233] op_sel_hi:[1,0]\n"
    "v_pk_mul_f32 v[176:177], v[176:177], v[232:233] op_sel_hi:[1,0]\n"
    "v_pk_mul_f32 v[178:179], v[178:179], v[232:233] op_sel_hi:[1,0]\n"
    "v_pk_mul_f32 v[180:181], v[180:181], v[232:233] op_sel_hi:[1,0]\n"
    "v_pk_mul_f32 v[182:183], v[182:183], v[232:233] op_sel_hi:[1,0]\n"
    "v_pk_mul_f32 v[184:185], v[184:185], v[232:233] op_sel_hi:[1,0]\n"
    "v_pk_mul_f32 v[186:187], v[186:187], v[232:233] op_sel_hi:[1,0]\n"
    "v_pk_mul_f32 v[188:189], v[188:189], v[232:233] op_sel_hi:[1,0]\n"
    "v_pk_mul_f32 v[190:191], v[190:191], v[232:233] op_sel_hi:[1,0]\n"
    "s_waitcnt lgkmcnt(0)\n"
    "v_add_u32 v249, 17408, v248\n"
    "v_add_u32 v250, 19584, v248\n"
    "ds_read2_b64 v[128:131], v249 offset0:0 offset1:2\n"
    "ds_read2_b64 v[132:135], v249 offset0:136 offset1:138\n"
    "ds_read2_b64 v[136:139], v250 offset0:0 offset1:2\n"
    "ds_read2_b64 v[140:143], v250 offset0:136 offset1:138\n"
    "v_cvt_pk_bf16_f32 v160, v160, v161\n"
    "v_cvt_pk_bf16_f32 v161, v162, v163\n"
    "v_cvt_pk_bf16_f32 v162, v164, v165\n"
    "v_cvt_pk_bf16_f32 v163, v166, v167\n"
    "ds_write_b128 v247, v[160:163] offset:0\n"
    "v_cvt_pk_bf16_f32 v168, v168, v169\n"
    "v_cvt_pk_bf16_f32 v169, v170, v171\n"
    "v_cvt_pk_bf16_f32 v170, v172, v173\n"
    "v_cvt_pk_bf16_f32 v171, v174, v175\n"
    "ds_write_b128 v247, v[168:171] offset:64\n"
    "v_cvt_pk_bf16_f32 v176, v176, v177\n"
    "v_cvt_pk_bf16_f32 v177, v178, v179\n"
    "v_cvt_pk_bf16_f32 v178, v180, v181\n"
    "v_cvt_pk_bf16_f32 v179, v182, v183\n"
    "ds_write_b128 v247, v[176:179] offset:128\n"
    "v_cvt_pk_bf16_f32 v184, v184, v185\n"
    "v_cvt_pk_bf16_f32 v185, v186, v187\n"
    "v_cvt_pk_bf16_f32 v186, v188, v189\n"
    "v_cvt_pk_bf16_f32 v187, v190, v191\n"
    "ds_write_b128 v247, v[184:187] offset:192\n"
    "s_mov_b32 s52, 0x4e800000\n"
    "v_cmp_nge_f32 s[54:55], s52, v240\n"
    "s_nop 3\n"
    "s_cmp_lg_u64 s[54:55], 0\n"
    "s_cselect_b32 %0, 1, 0\n"
    "s_waitcnt lgkmcnt(0)\n"
    "v_add_u32 v249, 0, v248\n"
    "v_add_u32 v250, 2176, v248\n"
    "ds_read2_b64 v[160:163], v249 offset0:0 offset1:2\n"
    "ds_read2_b64 v[164:167], v249 offset0:136 offset1:138\n"
    "ds_read2_b64 v[168:171], v250 offset0:0 offset1:2\n"
    "ds_read2_b64 v[172:175], v250 offset0:136 offset1:138\n"
    "s_waitcnt lgkmcnt(8)\n"
    "s_add_u32 s43, s42, 16384\n"
    "buffer_store_dwordx4 v[128:131], v229, s[96:99], s43 offen sc0 sc1\n"
    "buffer_store_dwordx4 v[132:135], v229, s[96:99], s43 offen offset:1024 sc0 sc1\n"
    "buffer_store_dwordx4 v[136:139], v229, s[96:99], s43 offen offset:2048 sc0 sc1\n"
    "buffer_store_dwordx4 v[140:143], v229, s[96:99], s43 offen offset:3072 sc0 sc1\n"
    "s_waitcnt lgkmcnt(0)\n"
    "s_add_u32 s43, s42, 20480\n"
    "buffer_store_dwordx4 v[160:163], v229, s[96:99], s43 offen sc0 sc1\n"
    "buffer_store_dwordx4 v[164:167], v229, s[96:99], s43 offen offset:1024 sc0 sc1\n"
    "buffer_store_dwordx4 v[168:171], v229, s[96:99], s43 offen offset:2048 sc0 sc1\n"
    "buffer_store_dwordx4 v[172:175], v229, s[96:99], s43 offen offset:3072 sc0 sc1\n"
    "s_branch END%=\n"
    "LOOPB%=:\n"
    "s_waitcnt lgkmcnt(5)\n"
    "v_mfma_f32_16x16x32_bf16 v[192:195], a[96:99], a[0:3], 0\n"
    "v_exp_f32 v200, v200\n"
    "v_mfma_f32_16x16x32_bf16 v[192:195], a[100:103], a[4:7], v[192:195]\n"
    "v_cvt_pk_bf16_f32 v211, v198, v199\n"
    "v_add_f32 v220, v220, v200\n"
    "v_mfma_f32_16x16x32_bf16 v[192:195], a[104:107], a[8:11], v[192:195]\n"
    "v_exp_f32 v201, v201\n"
    "v_mfma_f32_16x16x32_bf16 v[192:195], a[108:111], a[12:15], v[192:195]\n"
    "ds_read_b64_tr_b16 a[170:171], v224 offset:29184\n"
    "v_add_f32 v220, v220, v201\n"
    "v_mfma_f32_16x16x32_bf16 v[196:199], a[112:115], a[0:3], 0\n"
    "v_exp_f32 v202, v202\n"
    "s_add_u32 m0, s47, 12288\n"
    "v_mfma_f32_16x16x32_bf16 v[196:199], a[116:119], a[4:7], v[196:199]\n"
    "v_cvt_pk_bf16_f32 v212, v200, v201\n"
    "v_add_f32 v220, v220, v202\n"
    "v_mfma_f32_16x16x32_bf16 v[196:199], a[120:123], a[8:11], v[196:199]\n"
    "v_exp_f32 v203, v203\n"
    "buffer_load_dwordx4 v229, s[24:27], s49 offen nt lds\n"
    "buffer_load_dwordx4 v230, s[24:27], s49 offen offset:1024 nt lds\n"
    "buffer_load_dwordx4 v229, s[24:27], s49 offen offset:2048 nt lds\n"
    "buffer_load_dwordx4 v230, s[24:27], s49 offen offset:3072 nt lds\n"
    "v_mfma_f32_16x16x32_bf16 v[196:199], a[124:127], a[12:15], v[196:199]\n"
    "ds_read_b64_tr_b16 a[172:173], v224 offset:25344\n"
    "v_add_f32 v220, v220, v203\n"
    "v_mfma_f32_16x16x32_bf16 v[96:99], a[192:195], v[208:211], v[96:99]\n"
    "v_exp_f32 v204, v204\n"
    "s_add_u32 s48, s48, 8192\n"
    "v_mfma_f32_16x16x32_bf16 v[100:103], a[196:199], v[208:211], v[100:103]\n"
    "v_cvt_pk_bf16_f32 v213, v202, v203\n"
    "v_add_f32 v220, v220, v204\n"
    "ds_read_b64_tr_b16 a[178:179], v224 offset:29696\n"
    "v_mfma_f32_16x16x32_bf16 v[104:107], a[200:203], v[208:211], v[104:107]\n"
    "v_exp_f32 v205, v205\n"
    "s_add_u32 s47, s47, s45\n"
    "v_mfma_f32_16x16x32_bf16 v[108:111], a[204:207], v[208:211], v[108:111]\n"
    "ds_read_b64_tr_b16 a[174:175], v224 offset:29440\n"
    "v_add_f32 v220, v220, v205\n"
    "v_mfma_f32_16x16x32_bf16 v[112:115], a[208:211], v[208:211], v[112:115]\n"
    "v_exp_f32 v206, v206\n"
    "s_and_b32 s47, s47, 131071\n"
    "v_mfma_f32_16x16x32_bf16 v[116:119], a[212:215], v[208:211], v[116:119]\n"
    "v_cvt_pk_bf16_f32 v214, v204, v205\n"
    "v_add_f32 v220, v220, v206\n"
    "v_mfma_f32_16x16x32_bf16 v[120:123], a[216:219], v[208:211], v[120:123]\n"
    "v_exp_f32 v207, v207\n"
    "s_and_b32 s48, s48, 131071\n"
    "v_mfma_f32_16x16x32_bf16 v[124:127], a[220:223], v[208:211], v[124:127]\n"
    "ds_read_b64_tr_b16 a[176:177], v224 offset:25600\n"
    "v_add_f32 v220, v220, v207\n"
    "v_mfma_f32_16x16x32_bf16 v[200:203], a[96:99], a[16:19], 0\n"
    "v_exp_f32 v192, v192\n"
    "v_mfma_f32_16x16x32_bf16 v[200:203], a[100:103], a[20:23], v[200:203]\n"
    "v_cvt_pk_bf16_f32 v215, v206, v207\n"
    "v_add_f32 v216, v216, v192\n"
    "v_mfma_f32_16x16x32_bf16 v[200:203], a[104:107], a[24:27], v[200:203]\n"
    "v_exp_f32 v193, v193\n"
    "v_mfma_f32_16x16x32_bf16 v[200:203], a[108:111], a[28:31], v[200:203]\n"
    "ds_read_b64_tr_b16 a[180:181], v224 offset:25856\n"
    "v_add_f32 v216, v216, v193\n"
    "v_mfma_f32_16x16x32_bf16 v[204:207], a[112:115], a[16:19], 0\n"
    "v_exp_f32 v194, v194\n"
    "v_mfma_f32_16x16x32_bf16 v[204:207], a[116:119], a[20:23], v[204:207]\n"
    "v_cvt_pk_bf16_f32 v208, v192, v193\n"
    "v_add_f32 v216, v216, v194\n"
    "v_mfma_f32_16x16x32_bf16 v[204:207], a[120:123], a[24:27], v[204:207]\n"
    "v_exp_f32 v195, v195\n"
    "v_mfma_f32_16x16x32_bf16 v[204:207], a[124:127], a[28:31], v[204:207]\n"
    "ds_read_b64_tr_b16 a[182:183], v224 offset:29952\n"
    "v_add_f32 v216, v216, v195\n"
    "v_mfma_f32_16x16x32_bf16 v[128:131], a[192:195], v[212:215], v[128:131]\n"
    "v_exp_f32 v196, v196\n"
    "v_mfma_f32_16x16x32_bf16 v[132:135], a[196:199], v[212:215], v[132:135]\n"
    "v_cvt_pk_bf16_f32 v209, v194, v195\n"
    "v_add_f32 v216, v216, v196\n"
    "ds_read_b64_tr_b16 a[188:189], v224 offset:26368\n"
    "v_mfma_f32_16x16x32_bf16 v[136:139], a[200:203], v[212:215], v[136:139]\n"
    "v_exp_f32 v197, v197\n"
    "v_mfma_f32_16x16x32_bf16 v[140:143], a[204:207], v[212:215], v[140:143]\n"
    "ds_read_b64_tr_b16 a[184:185], v224 offset:26112\n"
    "v_add_f32 v216, v216, v197\n"
    "v_mfma_f32_16x16x32_bf16 v[144:147], a[208:211], v[212:215], v[144:147]\n"
    "v_exp_f32 v198, v198\n"
    "v_mfma_f32_16x16x32_bf16 v[148:151], a[212:215], v[212:215], v[148:151]\n"
    "v_cvt_pk_bf16_f32 v210, v196, v197\n"
    "v_add_f32 v216, v216, v198\n"
    "ds_read_b64_tr_b16 a[190:191], v224 offset:30464\n"
    "v_mfma_f32_16x16x32_bf16 v[152:155], a[216:219], v[212:215], v[152:155]\n"
    "v_exp_f32 v199, v199\n"
    "v_mfma_f32_16x16x32_bf16 v[156:159], a[220:223], v[212:215], v[156:159]\n"
    "ds_read_b64_tr_b16 a[186:187], v224 offset:30208\n"
    "v_add_f32 v216, v216, v199\n"
    "v_add_u32 v222, s46, v237\n"
    "v_add_u32 v223, s46, v238\n"
    "v_add_u32 v224, s46, v239\n"
    "ENTB%=:\n"
    "s_bitcmp1_b32 s53, s44\n"
    "s_cbranch_scc1 WBB%=\n"
    "WRB%=:\n"
    "v_mfma_f32_16x16x32_bf16 v[192:195], a[96:99], a[32:35], 0\n"
    "v_exp_f32 v200, v200\n"
    "v_mfma_f32_16x16x32_bf16 v[192:195], a[100:103], a[36:39], v[192:195]\n"
    "v_cvt_pk_bf16_f32 v211, v198, v199\n"
    "v_add_f32 v217, v217, v200\n"
    "v_mfma_f32_16x16x32_bf16 v[192:195], a[104:107], a[40:43], v[192:195]\n"
    "v_exp_f32 v201, v201\n"
    "s_add_u32 s49, s48, 4096\n"
    "v_mfma_f32_16x16x32_bf16 v[192:195], a[108:111], a[44:47], v[192:195]\n"
    "ds_read_b128 a[128:131], v222 offset:0\n"
    "v_add_f32 v217, v217, v201\n"
    "v_mfma_f32_16x16x32_bf16 v[196:199], a[112:115], a[32:35], 0\n"
    "v_exp_f32 v202, v202\n"
    "s_add_u32 m0, s47, 0\n"
    "v_mfma_f32_16x16x32_bf16 v[196:199], a[116:119], a[36:39], v[196:199]\n"
    "v_cvt_pk_bf16_f32 v212, v200, v201\n"
    "v_add_f32 v217, v217, v202\n"
    "v_mfma_f32_16x16x32_bf16 v[196:199], a[120:123], a[40:43], v[196:199]\n"
    "v_exp_f32 v203, v203\n"
    "buffer_load_dwordx4 v225, s[20:23], s48 offen nt lds\n"
    "buffer_load_dwordx4 v226, s[20:23], s48 offen offset:1024 nt lds\n"
    "buffer_load_dwordx4 v227, s[20:23], s48 offen offset:2048 nt lds\n"
    "buffer_load_dwordx4 v228, s[20:23], s48 offen offset:3072 nt lds\n"
    "v_mfma_f32_16x16x32_bf16 v[196:199], a[124:127], a[44:47], v[196:199]\n"
    "ds_read_b128 a[132:135], v223 offset:0\n"
    "v_add_f32 v217, v217, v203\n"
    "s_waitcnt lgkmcnt(2)\n"
    "v_mfma_f32_16x16x32_bf16 v[0:3], a[160:163], v[208:211], v[0:3]\n"
    "v_exp_f32 v204, v204\n"
    "v_mfma_f32_16x16x32_bf16 v[4:7], a[164:167], v[208:211], v[4:7]\n"
    "v_cvt_pk_bf16_f32 v213, v202, v203\n"
    "v_add_f32 v217, v217, v204\n"
    "v_mfma_f32_16x16x32_bf16 v[8:11], a[168:171], v[208:211], v[8:11]\n"
    "v_exp_f32 v205, v205\n"
    "v_mfma_f32_16x16x32_bf16 v[12:15], a[172:175], v[208:211], v[12:15]\n"
    "ds_read_b128 a[136:139], v222 offset:2048\n"
    "v_add_f32 v217, v217, v205\n"
    "v_mfma_f32_16x16x32_bf16 v[16:19], a[176:179], v[208:211], v[16:19]\n"
    "v_exp_f32 v206, v206\n"
    "v_mfma_f32_16x16x32_bf16 v[20:23], a[180:183], v[208:211], v[20:23]\n"
    "v_cvt_pk_bf16_f32 v214, v204, v205\n"
    "v_add_f32 v217, v217, v206\n"
    "v_mfma_f32_16x16x32_bf16 v[24:27], a[184:187], v[208:211], v[24:27]\n"
    "v_exp_f32 v207, v207\n"
    "v_mfma_f32_16x16x32_bf16 v[28:31], a[188:191], v[208:211], v[28:31]\n"
    "ds_read_b128 a[140:143], v223 offset:2048\n"
    "v_add_f32 v217, v217, v207\n"
    "v_mfma_f32_16x16x32_bf16 v[200:203], a[96:99], a[48:51], 0\n"
    "v_exp_f32 v192, v192\n"
    "v_mfma_f32_16x16x32_bf16 v[200:203], a[100:103], a[52:55], v[200:203]\n"
    "v_cvt_pk_bf16_f32 v215, v206, v207\n"
    "v_add_f32 v218, v218, v192\n"
    "v_mfma_f32_16x16x32_bf16 v[200:203], a[104:107], a[56:59], v[200:203]\n"
    "v_exp_f32 v193, v193\n"
    "s_add_u32 s46, s46, 32768\n"
    "v_mfma_f32_16x16x32_bf16 v[200:203], a[108:111], a[60:63], v[200:203]\n"
    "ds_read_b128 a[144:147], v222 offset:4096\n"
    "v_add_f32 v218, v218, v193\n"
    "v_mfma_f32_16x16x32_bf16 v[204:207], a[112:115], a[48:51], 0\n"
    "v_exp_f32 v194, v194\n"
    "s_add_u32 m0, s47, 4096\n"
    "v_mfma_f32_16x16x32_bf16 v[204:207], a[116:119], a[52:55], v[204:207]\n"
    "v_cvt_pk_bf16_f32 v208, v192, v193\n"
    "v_add_f32 v218, v218, v194\n"
    "v_mfma_f32_16x16x32_bf16 v[204:207], a[120:123], a[56:59], v[204:207]\n"
    "v_exp_f32 v195, v195\n"
    "buffer_load_dwordx4 v225, s[20:23], s49 offen nt lds\n"
    "buffer_load_dwordx4 v226, s[20:23], s49 offen offset:1024 nt lds\n"
    "buffer_load_dwordx4 v227, s[20:23], s49 offen offset:2048 nt lds\n"
    "buffer_load_dwordx4 v228, s[20:23], s49 offen offset:3072 nt lds\n"
    "s_and_b32 s46, s46, 131071\n"
    "v_mfma_f32_16x16x32_bf16 v[204:207], a[124:127], a[60:63], v[204:207]\n"
    "ds_read_b128 a[148:151], v223 offset:4096\n"
    "v_add_f32 v218, v218, v195\n"
    "v_mfma_f32_16x16x32_bf16 v[32:35], a[160:163], v[212:215], v[32:35]\n"
    "v_exp_f32 v196, v196\n"
    "v_mfma_f32_16x16x32_bf16 v[36:39], a[164:167], v[212:215], v[36:39]\n"
    "v_cvt_pk_bf16_f32 v209, v194, v195\n"
    "v_add_f32 v218, v218, v196\n"
    "v_mfma_f32_16x16x32_bf16 v[40:43], a[168:171], v[212:215], v[40:43]\n"
    "v_exp_f32 v197, v197\n"
    "v_mfma_f32_16x16x32_bf16 v[44:47], a[172:175], v[212:215], v[44:47]\n"
    "ds_read_b128 a[152:155], v222 offset:6144\n"
    "v_add_f32 v218, v218, v197\n"
    "v_mfma_f32_16x16x32_bf16 v[48:51], a[176:179], v[212:215], v[48:51]\n"
    "v_exp_f32 v198, v198\n"
    "v_mfma_f32_16x16x32_bf16 v[52:55], a[180:183], v[212:215], v[52:55]\n"
    "v_cvt_pk_bf16_f32 v210, v196, v197\n"
    "v_add_f32 v218, v218, v198\n"
    "v_mfma_f32_16x16x32_bf16 v[56:59], a[184:187], v[212:215], v[56:59]\n"
    "v_exp_f32 v199, v199\n"
    "v_mfma_f32_16x16x32_bf16 v[60:63], a[188:191], v[212:215], v[60:63]\n"
    "ds_read_b128 a[156:159], v223 offset:6144\n"
    "v_add_f32 v218, v218, v199\n"
    "v_mfma_f32_16x16x32_bf16 v[192:195], a[96:99], a[64:67], 0\n"
    "v_exp_f32 v200, v200\n"
    "v_mfma_f32_16x16x32_bf16 v[192:195], a[100:103], a[68:71], v[192:195]\n"
    "v_cvt_pk_bf16_f32 v211, v198, v199\n"
    "v_add_f32 v219, v219, v200\n"
    "v_mfma_f32_16x16x32_bf16 v[192:195], a[104:107], a[72:75], v[192:195]\n"
    "v_exp_f32 v201, v201\n"
    "v_mfma_f32_16x16x32_bf16 v[192:195], a[108:111], a[76:79], v[192:195]\n"
    "ds_read_b64_tr_b16 a[192:193], v224 offset:8192\n"
    "v_add_f32 v219, v219, v201\n"
    "v_mfma_f32_16x16x32_bf16 v[196:199], a[112:115], a[64:67], 0\n"
    "v_exp_f32 v202, v202\n"
    "s_add_u32 m0, s47, 8192\n"
    "v_mfma_f32_16x16x32_bf16 v[196:199], a[116:119], a[68:71], v[196:199]\n"
    "v_cvt_pk_bf16_f32 v212, v200, v201\n"
    "v_add_f32 v219, v219, v202\n"
    "v_mfma_f32_16x16x32_bf16 v[196:199], a[120:123], a[72:75], v[196:199]\n"
    "v_exp_f32 v203, v203\n"
    "buffer_load_dwordx4 v229, s[24:27], s48 offen nt lds\n"
    "buffer_load_dwordx4 v230, s[24:27], s48 offen offset:1024 nt lds\n"
    "buffer_load_dwordx4 v229, s[24:27], s48 offen offset:2048 nt lds\n"
    "buffer_load_dwordx4 v230, s[24:27], s48 offen offset:3072 nt lds\n"
    "v_mfma_f32_16x16x32_bf16 v[196:199], a[124:127], a[76:79], v[196:199]\n"
    "ds_read_b64_tr_b16 a[194:195], v224 offset:12288\n"
    "v_add_f32 v219, v219, v203\n"
    "v_mfma_f32_16x16x32_bf16 v[64:67], a[160:163], v[208:211], v[64:67]\n"
    "v_exp_f32 v204, v204\n"
    "v_mfma_f32_16x16x32_bf16 v[68:71], a[164:167], v[208:211], v[68:71]\n"
    "v_cvt_pk_bf16_f32 v213, v202, v203\n"
    "v_add_f32 v219, v219, v204\n"
    "ds_read_b64_tr_b16 a[200:201], v224 offset:8704\n"
    "v_mfma_f32_16x16x32_bf16 v[72:75], a[168:171], v[208:211], v[72:75]\n"
    "v_exp_f32 v205, v205\n"
    "v_mfma_f32_16x16x32_bf16 v[76:79], a[172:175], v[208:211], v[76:79]\n"
    "ds_read_b64_tr_b16 a[196:197], v224 offset:8448\n"
    "v_add_f32 v219, v219, v205\n"
    "v_mfma_f32_16x16x32_bf16 v[80:83], a[176:179], v[208:211], v[80:83]\n"
    "v_exp_f32 v206, v206\n"
    "v_mfma_f32_16x16x32_bf16 v[84:87], a[180:183], v[208:211], v[84:87]\n"
    "v_cvt_pk_bf16_f32 v214, v204, v205\n"
    "v_add_f32 v219, v219, v206\n"
    "v_mfma_f32_16x16x32_bf16 v[88:91], a[184:187], v[208:211], v[88:91]\n"
    "v_exp_f32 v207, v207\n"
    "v_mfma_f32_16x16x32_bf16 v[92:95], a[188:191], v[208:211], v[92:95]\n"
    "ds_read_b64_tr_b16 a[198:199], v224 offset:12544\n"
    "v_add_f32 v219, v219, v207\n"
    "s_waitcnt lgkmcnt(5)\n"
    "v_mfma_f32_16x16x32_bf16 v[200:203], a[128:131], a[0:3], 0\n"
    "v_exp_f32 v192, v192\n"
    "v_mfma_f32_16x16x32_bf16 v[200:203], a[132:135], a[4:7], v[200:203]\n"
    "v_cvt_pk_bf16_f32 v215, v206, v207\n"
    "v_add_f32 v220, v220, v192\n"
    "v_mfma_f32_16x16x32_bf16 v[200:203], a[136:139], a[8:11], v[200:203]\n"
    "v_exp_f32 v193, v193\n"
    "v_mfma_f32_16x16x32_bf16 v[200:203], a[140:143], a[12:15], v[200:203]\n"
    "ds_read_b64_tr_b16 a[202:203], v224 offset:12800\n"
    "v_add_f32 v220, v220, v193\n"
    "v_mfma_f32_16x16x32_bf16 v[204:207], a[144:147], a[0:3], 0\n"
    "v_exp_f32 v194, v194\n"
    "s_add_u32 m0, s47, 12288\n"
    "v_mfma_f32_16x16x32_bf16 v[204:207], a[148:151], a[4:7], v[204:207]\n"
    "v_cvt_pk_bf16_f32 v208, v192, v193\n"
    "v_add_f32 v220, v220, v194\n"
    "v_mfma_f32_16x16x32_bf16 v[204:207], a[152:155], a[8:11], v[204:207]\n"
    "v_exp_f32 v195, v195\n"
    "buffer_load_dwordx4 v229, s[24:27], s49 offen nt lds\n"
    "buffer_load_dwordx4 v230, s[24:27], s49 offen offset:1024 nt lds\n"
    "buffer_load_dwordx4 v229, s[24:27], s49 offen offset:2048 nt lds\n"
    "buffer_load_dwordx4 v230, s[24:27], s49 offen offset:3072 nt lds\n"
    "v_mfma_f32_16x16x32_bf16 v[204:207], a[156:159], a[12:15], v[204:207]\n"
    "ds_read_b64_tr_b16 a[204:205], v224 offset:8960\n"
    "v_add_f32 v220, v220, v195\n"
    "v_mfma_f32_16x16x32_bf16 v[96:99], a[160:163], v[212:215], v[96:99]\n"
    "v_exp_f32 v196, v196\n"
    "s_add_u32 s48, s48, 8192\n"
    "v_mfma_f32_16x16x32_bf16 v[100:103], a[164:167], v[212:215], v[100:103]\n"
    "v_cvt_pk_bf16_f32 v209, v194, v195\n"
    "v_add_f32 v220, v220, v196\n"
    "ds_read_b64_tr_b16 a[210:211], v224 offset:13312\n"
    "v_mfma_f32_16x16x32_bf16 v[104:107], a[168:171], v[212:215], v[104:107]\n"
    "v_exp_f32 v197, v197\n"
    "s_add_u32 s47, s47, s45\n"
    "v_mfma_f32_16x16x32_bf16 v[108:111], a[172:175], v[212:215], v[108:111]\n"
    "ds_read_b64_tr_b16 a[206:207], v224 offset:13056\n"
    "v_add_f32 v220, v220, v197\n"
    "v_mfma_f32_16x16x32_bf16 v[112:115], a[176:179], v[212:215], v[112:115]\n"
    "v_exp_f32 v198, v198\n"
    "s_and_b32 s47, s47, 131071\n"
    "v_mfma_f32_16x16x32_bf16 v[116:119], a[180:183], v[212:215], v[116:119]\n"
    "v_cvt_pk_bf16_f32 v210, v196, v197\n"
    "v_add_f32 v220, v220, v198\n"
    "v_mfma_f32_16x16x32_bf16 v[120:123], a[184:187], v[212:215], v[120:123]\n"
    "v_exp_f32 v199, v199\n"
    "s_and_b32 s48, s48, 131071\n"
    "v_mfma_f32_16x16x32_bf16 v[124:127], a[188:191], v[212:215], v[124:127]\n"
    "ds_read_b64_tr_b16 a[208:209], v224 offset:9216\n"
    "v_add_f32 v220, v220, v199\n"
    "v_mfma_f32_16x16x32_bf16 v[192:195], a[128:131], a[16:19], 0\n"
    "v_exp_f32 v200, v200\n"
    "v_mfma_f32_16x16x32_bf16 v[192:195], a[132:135], a[20:23], v[192:195]\n"
    "v_cvt_pk_bf16_f32 v211, v198, v199\n"
    "v_add_f32 v216, v216, v200\n"
    "v_mfma_f32_16x16x32_bf16 v[192:195], a[136:139], a[24:27], v[192:195]\n"
    "v_exp_f32 v201, v201\n"
    "v_mfma_f32_16x16x32_bf16 v[192:195], a[140:143], a[28:31], v[192:195]\n"
    "ds_read_b64_tr_b16 a[212:213], v224 offset:9472\n"
    "v_add_f32 v216, v216, v201\n"
    "v_mfma_f32_16x16x32_bf16 v[196:199], a[144:147], a[16:19], 0\n"
    "v_exp_f32 v202, v202\n"
    "v_mfma_f32_16x16x32_bf16 v[196:199], a[148:151], a[20:23], v[196:199]\n"
    "v_cvt_pk_bf16_f32 v212, v200, v201\n"
    "v_add_f32 v216, v216, v202\n"
    "v_mfma_f32_16x16x32_bf16 v[196:199], a[152:155], a[24:27], v[196:199]\n"
    "v_exp_f32 v203, v203\n"
    "v_mfma_f32_16x16x32_bf16 v[196:199], a[156:159], a[28:31], v[196:199]\n"
    "ds_read_b64_tr_b16 a[214:215], v224 offset:13568\n"
    "v_add_f32 v216, v216, v203\n"
    "v_mfma_f32_16x16x32_bf16 v[128:131], a[160:163], v[208:211], v[128:131]\n"
    "v_exp_f32 v204, v204\n"
    "v_mfma_f32_16x16x32_bf16 v[132:135], a[164:167], v[208:211], v[132:135]\n"
    "v_cvt_pk_bf16_f32 v213, v202, v203\n"
    "v_add_f32 v216, v216, v204\n"
    "ds_read_b64_tr_b16 a[220:221], v224 offset:9984\n"
    "v_mfma_f32_16x16x32_bf16 v[136:139], a[168:171], v[208:211], v[136:139]\n"
    "v_exp_f32 v205, v205\n"
    "v_mfma_f32_16x16x32_bf16 v[140:143], a[172:175], v[208:211], v[140:143]\n"
    "ds_read_b64_tr_b16 a[216:217], v224 offset:9728\n"
    "v_add_f32 v216, v216, v205\n"
    "v_mfma_f32_16x16x32_bf16 v[144:147], a[176:179], v[208:211], v[144:147]\n"
    "v_exp_f32 v206, v206\n"
    "v_mfma_f32_16x16x32_bf16 v[148:151], a[180:183], v[208:211], v[148:151]\n"
    "v_cvt_pk_bf16_f32 v214, v204, v205\n"
    "v_add_f32 v216, v216, v206\n"
    "ds_read_b64_tr_b16 a[222:223], v224 offset:14080\n"
    "v_mfma_f32_16x16x32_bf16 v[152:155], a[184:187], v[208:211], v[152:155]\n"
    "v_exp_f32 v207, v207\n"
    "v_mfma_f32_16x16x32_bf16 v[156:159], a[188:191], v[208:211], v[156:159]\n"
    "ds_read_b64_tr_b16 a[218:219], v224 offset:13824\n"
    "v_add_f32 v216, v216, v207\n"
    "s_waitcnt vmcnt(16)\n"
    "s_barrier\n"
    "v_mfma_f32_16x16x32_bf16 v[200:203], a[128:131], a[32:35], 0\n"
    "v_exp_f32 v192, v192\n"
    "v_mfma_f32_16x16x32_bf16 v[200:203], a[132:135], a[36:39], v[200:203]\n"
    "v_cvt_pk_bf16_f32 v215, v206, v207\n"
    "v_add_f32 v217, v217, v192\n"
    "v_mfma_f32_16x16x32_bf16 v[200:203], a[136:139], a[40:43], v[200:203]\n"
    "v_exp_f32 v193, v193\n"
    "s_add_u32 s49, s48, 4096\n"
    "v_mfma_f32_16x16x32_bf16 v[200:203], a[140:143], a[44:47], v[200:203]\n"
    "ds_read_b128 a[96:99], v222 offset:16384\n"
    "v_add_f32 v217, v217, v193\n"
    "v_mfma_f32_16x16x32_bf16 v[204:207], a[144:147], a[32:35], 0\n"
    "v_exp_f32 v194, v194\n"
    "s_add_u32 m0, s47, 0\n"
    "v_mfma_f32_16x16x32_bf16 v[204:207], a[148:151], a[36:39], v[204:207]\n"
    "v_cvt_pk_bf16_f32 v208, v192, v193\n"
    "v_add_f32 v217, v217, v194\n"
    "v_mfma_f32_16x16x32_bf16 v[204:207], a[152:155], a[40:43], v[204:207]\n"
    "v_exp_f32 v195, v195\n"
    "buffer_load_dwordx4 v225, s[20:23], s48 offen nt lds\n"
    "buffer_load_dwordx4 v226, s[20:23], s48 offen offset:1024 nt lds\n"
    "buffer_load_dwordx4 v227, s[20:23], s48 offen offset:2048 nt lds\n"
    "buffer_load_dwordx4 v228, s[20:23], s48 offen offset:3072 nt lds\n"
    "v_mfma_f32_16x16x32_bf16 v[204:207], a[156:159], a[44:47], v[204:207]\n"
    "ds_read_b128 a[100:103], v223 offset:16384\n"
    "v_add_f32 v217, v217, v195\n"
    "s_waitcnt lgkmcnt(2)\n"
    "v_mfma_f32_16x16x32_bf16 v[0:3], a[192:195], v[212:215], v[0:3]\n"
    "v_exp_f32 v196, v196\n"
    "v_mfma_f32_16x16x32_bf16 v[4:7], a[196:199], v[212:215], v[4:7]\n"
    "v_cvt_pk_bf16_f32 v209, v194, v195\n"
    "v_add_f32 v217, v217, v196\n"
    "v_mfma_f32_16x16x32_bf16 v[8:11], a[200:203], v[212:215], v[8:11]\n"
    "v_exp_f32 v197, v197\n"
    "v_mfma_f32_16x16x32_bf16 v[12:15], a[204:207], v[212:215], v[12:15]\n"
    "ds_read_b128 a[104:107], v222 offset:18432\n"
    "v_add_f32 v217, v217, v197\n"
    "v_mfma_f32_16x16x32_bf16 v[16:19], a[208:211], v[212:215], v[16:19]\n"
    "v_exp_f32 v198, v198\n"
    "v_mfma_f32_16x16x32_bf16 v[20:23], a[212:215], v[212:215], v[20:23]\n"
    "v_cvt_pk_bf16_f32 v210, v196, v197\n"
    "v_add_f32 v217, v217, v198\n"
    "v_mfma_f32_16x16x32_bf16 v[24:27], a[216:219], v[212:215], v[24:27]\n"
    "v_exp_f32 v199, v199\n"
    "v_mfma_f32_16x16x32_bf16 v[28:31], a[220:223], v[212:215], v[28:31]\n"
    "ds_read_b128 a[108:111], v223 offset:18432\n"
    "v_add_f32 v217, v217, v199\n"
    "v_mfma_f32_16x16x32_bf16 v[192:195], a[128:131], a[48:51], 0\n"
    "v_exp_f32 v200, v200\n"
    "v_mfma_f32_16x16x32_bf16 v[192:195], a[132:135], a[52:55], v[192:195]\n"
    "v_cvt_pk_bf16_f32 v211, v198, v199\n"
    "v_add_f32 v218, v218, v200\n"
    "v_mfma_f32_16x16x32_bf16 v[192:195], a[136:139], a[56:59], v[192:195]\n"
    "v_exp_f32 v201, v201\n"
    "s_add_u32 s44, s44, 2\n"
    "v_mfma_f32_16x16x32_bf16 v[192:195], a[140:143], a[60:63], v[192:195]\n"
    "ds_read_b128 a[112:115], v222 offset:20480\n"
    "v_add_f32 v218, v218, v201\n"
    "v_mfma_f32_16x16x32_bf16 v[196:199], a[144:147], a[48:51], 0\n"
    "v_exp_f32 v202, v202\n"
    "s_add_u32 m0, s47, 4096\n"
    "v_mfma_f32_16x16x32_bf16 v[196:199], a[148:151], a[52:55], v[196:199]\n"
    "v_cvt_pk_bf16_f32 v212, v200, v201\n"
    "v_add_f32 v218, v218, v202\n"
    "v_mfma_f32_16x16x32_bf16 v[196:199], a[152:155], a[56:59], v[196:199]\n"
    "v_exp_f32 v203, v203\n"
    "buffer_load_dwordx4 v225, s[20:23], s49 offen nt lds\n"
    "buffer_load_dwordx4 v226, s[20:23], s49 offen offset:1024 nt lds\n"
    "buffer_load_dwordx4 v227, s[20:23], s49 offen offset:2048 nt lds\n"
    "buffer_load_dwordx4 v228, s[20:23], s49 offen offset:3072 nt lds\n"
    "v_mfma_f32_16x16x32_bf16 v[196:199], a[156:159], a[60:63], v[196:199]\n"
    "ds_read_b128 a[116:119], v223 offset:20480\n"
    "v_add_f32 v218, v218, v203\n"
    "v_mfma_f32_16x16x32_bf16 v[32:35], a[192:195], v[208:211], v[32:35]\n"
    "v_exp_f32 v204, v204\n"
    "v_mfma_f32_16x16x32_bf16 v[36:39], a[196:199], v[208:211], v[36:39]\n"
    "v_cvt_pk_bf16_f32 v213, v202, v203\n"
    "v_add_f32 v218, v218, v204\n"
    "v_mfma_f32_16x16x32_bf16 v[40:43], a[200:203], v[208:211], v[40:43]\n"
    "v_exp_f32 v205, v205\n"
    "v_mfma_f32_16x16x32_bf16 v[44:47], a[204:207], v[208:211], v[44:47]\n"
    "ds_read_b128 a[120:123], v222 offset:22528\n"
    "v_add_f32 v218, v218, v205\n"
    "v_mfma_f32_16x16x32_bf16 v[48:51], a[208:211], v[208:211], v[48:51]\n"
    "v_exp_f32 v206, v206\n"
    "v_mfma_f32_16x16x32_bf16 v[52:55], a[212:215], v[208:211], v[52:55]\n"
    "v_cvt_pk_bf16_f32 v214, v204, v205\n"
    "v_add_f32 v218, v218, v206\n"
    "v_mfma_f32_16x16x32_bf16 v[56:59], a[216:219], v[208:211], v[56:59]\n"
    "v_exp_f32 v207, v207\n"
    "v_mfma_f32_16x16x32_bf16 v[60:63], a[220:223], v[208:211], v[60:63]\n"
    "ds_read_b128 a[124:127], v223 offset:22528\n"
    "v_add_f32 v218, v218, v207\n"
    "v_mfma_f32_16x16x32_bf16 v[200:203], a[128:131], a[64:67], 0\n"
    "v_exp_f32 v192, v192\n"
    "v_mfma_f32_16x16x32_bf16 v[200:203], a[132:135], a[68:71], v[200:203]\n"
    "v_cvt_pk_bf16_f32 v215, v206, v207\n"
    "v_add_f32 v219, v219, v192\n"
    "v_mfma_f32_16x16x32_bf16 v[200:203], a[136:139], a[72:75], v[200:203]\n"
    "v_exp_f32 v193, v193\n"
    "v_mfma_f32_16x16x32_bf16 v[200:203], a[140:143], a[76:79], v[200:203]\n"
    "ds_read_b64_tr_b16 a[160:161], v224 offset:24576\n"
    "v_add_f32 v219, v219, v193\n"
    "v_mfma_f32_16x16x32_bf16 v[204:207], a[144:147], a[64:67], 0\n"
    "v_exp_f32 v194, v194\n"
    "s_add_u32 m0, s47, 8192\n"
    "v_mfma_f32_16x16x32_bf16 v[204:207], a[148:151], a[68:71], v[204:207]\n"
    "v_cvt_pk_bf16_f32 v208, v192, v193\n"
    "v_add_f32 v219, v219, v194\n"
    "v_mfma_f32_16x16x32_bf16 v[204:207], a[152:155], a[72:75], v[204:207]\n"
    "v_exp_f32 v195, v195\n"
    "buffer_load_dwordx4 v229, s[24:27], s48 offen nt lds\n"
    "buffer_load_dwordx4 v230, s[24:27], s48 offen offset:1024 nt lds\n"
    "buffer_load_dwordx4 v229, s[24:27], s48 offen offset:2048 nt lds\n"
    "buffer_load_dwordx4 v230, s[24:27], s48 offen offset:3072 nt lds\n"
    "v_mfma_f32_16x16x32_bf16 v[204:207], a[156:159], a[76:79], v[204:207]\n"
    "ds_read_b64_tr_b16 a[162:163], v224 offset:28672\n"
    "v_add_f32 v219, v219, v195\n"
    "v_mfma_f32_16x16x32_bf16 v[64:67], a[192:195], v[212:215], v[64:67]\n"
    "v_exp_f32 v196, v196\n"
    "v_mfma_f32_16x16x32_bf16 v[68:71], a[196:199], v[212:215], v[68:71]\n"
    "v_cvt_pk_bf16_f32 v209, v194, v195\n"
    "v_add_f32 v219, v219, v196\n"
    "ds_read_b64_tr_b16 a[168:169], v224 offset:25088\n"
    "v_mfma_f32_16x16x32_bf16 v[72:75], a[200:203], v[212:215], v[72:75]\n"
    "v_exp_f32 v197, v197\n"
    "v_mfma_f32_16x16x32_bf16 v[76:79], a[204:207], v[212:215], v[76:79]\n"
    "ds_read_b64_tr_b16 a[164:165], v224 offset:24832\n"
    "v_add_f32 v219, v219, v197\n"
    "v_mfma_f32_16x16x32_bf16 v[80:83], a[208:211], v[212:215], v[80:83]\n"
    "v_exp_f32 v198, v198\n"
    "v_mfma_f32_16x16x32_bf16 v[84:87], a[212:215], v[212:215], v[84:87]\n"
    "v_cvt_pk_bf16_f32 v210, v196, v197\n"
    "v_add_f32 v219, v219, v198\n"
    "v_mfma_f32_16x16x32_bf16 v[88:91], a[216:219], v[212:215], v[88:91]\n"
    "v_exp_f32 v199, v199\n"
    "v_mfma_f32_16x16x32_bf16 v[92:95], a[220:223], v[212:215], v[92:95]\n"
    "ds_read_b64_tr_b16 a[166:167], v224 offset:28928\n"
    "v_add_f32 v219, v219, v199\n"
    "s_cmp_eq_u32 s44, 14\n"
    "s_cbranch_scc0 LOOPB%=\n"
    "TRB%=:\n"
    "s_waitcnt vmcnt(0)\n"
    "s_barrier\n"
    "s_waitcnt lgkmcnt(5)\n"
    "v_mfma_f32_16x16x32_bf16 v[192:195], a[96:99], a[0:3], 0\n"
    "v_exp_f32 v200, v200\n"
    "v_mfma_f32_16x16x32_bf16 v[192:195], a[100:103], a[4:7], v[192:195]\n"
    "v_cvt_pk_bf16_f32 v211, v198, v199\n"
    "v_add_f32 v220, v220, v200\n"
    "ds_read_b128 a[128:131], v222 offset:32768\n"
    "v_mfma_f32_16x16x32_bf16 v[192:195], a[104:107], a[8:11], v[192:195]\n"
    "v_exp_f32 v201, v201\n"
    "v_mfma_f32_16x16x32_bf16 v[192:195], a[108:111], a[12:15], v[192:195]\n"
    "v_add_f32 v220, v220, v201\n"
    "ds_read_b128 a[132:135], v223 offset:32768\n"
    "ds_read_b128 a[136:139], v222 offset:34816\n"
    "v_mfma_f32_16x16x32_bf16 v[196:199], a[112:115], a[0:3], 0\n"
    "v_exp_f32 v202, v202\n"
    "v_mfma_f32_16x16x32_bf16 v[196:199], a[116:119], a[4:7], v[196:199]\n"
    "v_cvt_pk_bf16_f32 v212, v200, v201\n"
    "v_add_f32 v220, v220, v202\n"
    "ds_read_b128 a[140:143], v223 offset:34816\n"
    "v_mfma_f32_16x16x32_bf16 v[196:199], a[120:123], a[8:11], v[196:199]\n"
    "v_exp_f32 v203, v203\n"
    "v_mfma_f32_16x16x32_bf16 v[196:199], a[124:127], a[12:15], v[196:199]\n"
    "v_add_f32 v220, v220, v203\n"
    "ds_read_b128 a[144:147], v222 offset:36864\n"
    "ds_read_b128 a[148:151], v223 offset:36864\n"
    "v_mfma_f32_16x16x32_bf16 v[96:99], a[192:195], v[208:211], v[96:99]\n"
    "v_exp_f32 v204, v204\n"
    "v_mfma_f32_16x16x32_bf16 v[100:103], a[196:199], v[208:211], v[100:103]\n"
    "v_cvt_pk_bf16_f32 v213, v202, v203\n"
    "v_add_f32 v220, v220, v204\n"
    "ds_read_b128 a[152:155], v222 offset:38912\n"
    "v_mfma_f32_16x16x32_bf16 v[104:107], a[200:203], v[208:211], v[104:107]\n"
    "v_exp_f32 v205, v205\n"
    "v_mfma_f32_16x16x32_bf16 v[108:111], a[204:207], v[208:211], v[108:111]\n"
    "v_add_f32 v220, v220, v205\n"
    "ds_read_b128 a[156:159], v223 offset:38912\n"
    "ds_read_b64_tr_b16 a[170:171], v224 offset:29184\n"
    "v_mfma_f32_16x16x32_bf16 v[112:115], a[208:211], v[208:211], v[112:115]\n"
    "v_exp_f32 v206, v206\n"
    "v_mfma_f32_16x16x32_bf16 v[116:119], a[212:215], v[208:211], v[116:119]\n"
    "v_cvt_pk_bf16_f32 v214, v204, v205\n"
    "v_add_f32 v220, v220, v206\n"
    "ds_read_b64_tr_b16 a[172:173], v224 offset:25344\n"
    "v_mfma_f32_16x16x32_bf16 v[120:123], a[216:219], v[208:211], v[120:123]\n"
    "v_exp_f32 v207, v207\n"
    "v_mfma_f32_16x16x32_bf16 v[124:127], a[220:223], v[208:211], v[124:127]\n"
    "v_add_f32 v220, v220, v207\n"
    "ds_read_b64_tr_b16 a[174:175], v224 offset:29440\n"
    "ds_read_b64_tr_b16 a[176:177], v224 offset:25600\n"
    "ds_read_b64_tr_b16 a[178:179], v224 offset:29696\n"
    "s_waitcnt lgkmcnt(5)\n"
    "v_mfma_f32_16x16x32_bf16 v[200:203], a[128:131], a[0:3], 0\n"
    "v_exp_f32 v192, v192\n"
    "v_mfma_f32_16x16x32_bf16 v[200:203], a[132:135], a[4:7], v[200:203]\n"
    "v_cvt_pk_bf16_f32 v215, v206, v207\n"
    "v_add_f32 v216, v216, v192\n"
    "v_mfma_f32_16x16x32_bf16 v[200:203], a[136:139], a[8:11], v[200:203]\n"
    "v_exp_f32 v193, v193\n"
    "v_mfma_f32_16x16x32_bf16 v[200:203], a[140:143], a[12:15], v[200:203]\n"
    "ds_read_b64_tr_b16 a[180:181], v224 offset:25856\n"
    "v_add_f32 v216, v216, v193\n"
    "v_mfma_f32_16x16x32_bf16 v[204:207], a[144:147], a[0:3], 0\n"
    "v_exp_f32 v194, v194\n"
    "v_mfma_f32_16x16x32_bf16 v[204:207], a[148:151], a[4:7], v[204:207]\n"
    "v_cvt_pk_bf16_f32 v208, v192, v193\n"
    "v_add_f32 v216, v216, v194\n"
    "v_mfma_f32_16x16x32_bf16 v[204:207], a[152:155], a[8:11], v[204:207]\n"
    "v_exp_f32 v195, v195\n"
    "v_mfma_f32_16x16x32_bf16 v[204:207], a[156:159], a[12:15], v[204:207]\n"
    "ds_read_b64_tr_b16 a[182:183], v224 offset:29952\n"
    "v_add_f32 v216, v216, v195\n"
    "v_mfma_f32_16x16x32_bf16 v[128:131], a[192:195], v[212:215], v[128:131]\n"
    "v_exp_f32 v196, v196\n"
    "v_mfma_f32_16x16x32_bf16 v[132:135], a[196:199], v[212:215], v[132:135]\n"
    "v_cvt_pk_bf16_f32 v209, v194, v195\n"
    "v_add_f32 v216, v216, v196\n"
    "ds_read_b64_tr_b16 a[188:189], v224 offset:26368\n"
    "v_mfma_f32_16x16x32_bf16 v[136:139], a[200:203], v[212:215], v[136:139]\n"
    "v_exp_f32 v197, v197\n"
    "v_mfma_f32_16x16x32_bf16 v[140:143], a[204:207], v[212:215], v[140:143]\n"
    "ds_read_b64_tr_b16 a[184:185], v224 offset:26112\n"
    "v_add_f32 v216, v216, v197\n"
    "v_mfma_f32_16x16x32_bf16 v[144:147], a[208:211], v[212:215], v[144:147]\n"
    "v_exp_f32 v198, v198\n"
    "v_mfma_f32_16x16x32_bf16 v[148:151], a[212:215], v[212:215], v[148:151]\n"
    "v_cvt_pk_bf16_f32 v210, v196, v197\n"
    "v_add_f32 v216, v216, v198\n"
    "ds_read_b64_tr_b16 a[190:191], v224 offset:30464\n"
    "v_mfma_f32_16x16x32_bf16 v[152:155], a[216:219], v[212:215], v[152:155]\n"
    "v_exp_f32 v199, v199\n"
    "v_mfma_f32_16x16x32_bf16 v[156:159], a[220:223], v[212:215], v[156:159]\n"
    "ds_read_b64_tr_b16 a[186:187], v224 offset:30208\n"
    "v_add_f32 v216, v216, v199\n"
    "s_branch FINAL%=\n"
    "ENDB%=:\n"
    "s_nop 7\n"
    "v_pk_mul_f32 v[128:129], v[128:129], v[230:231] op_sel_hi:[1,0]\n"
    "v_pk_mul_f32 v[130:131], v[130:131], v[230:231] op_sel_hi:[1,0]\n"
    "v_pk_mul_f32 v[132:133], v[132:133], v[230:231] op_sel_hi:[1,0]\n"
    "v_pk_mul_f32 v[134:135], v[134:135], v[230:231] op_sel_hi:[1,0]\n"
    "v_pk_mul_f32 v[136:137], v[136:137], v[230:231] op_sel_hi:[1,0]\n"
    "v_pk_mul_f32 v[138:139], v[138:139], v[230:231] op_sel_hi:[1,0]\n"
    "v_pk_mul_f32 v[140:141], v[140:141], v[230:231] op_sel_hi:[1,0]\n"
    "v_pk_mul_f32 v[142:143], v[142:143], v[230:231] op_sel_hi:[1,0]\n"
    "v_pk_mul_f32 v[144:145], v[144:145], v[230:231] op_sel_hi:[1,0]\n"
    "v_pk_mul_f32 v[146:147], v[146:147], v[230:231] op_sel_hi:[1,0]\n"
    "v_pk_mul_f32 v[148:149], v[148:149], v[230:231] op_sel_hi:[1,0]\n"
    "v_pk_mul_f32 v[150:151], v[150:151], v[230:231] op_sel_hi:[1,0]\n"
    "v_pk_mul_f32 v[152:153], v[152:153], v[230:231] op_sel_hi:[1,0]\n"
    "v_pk_mul_f32 v[154:155], v[154:155], v[230:231] op_sel_hi:[1,0]\n"
    "v_pk_mul_f32 v[156:157], v[156:157], v[230:231] op_sel_hi:[1,0]\n"
    "v_pk_mul_f32 v[158:159], v[158:159], v[230:231] op_sel_hi:[1,0]\n"
    "s_waitcnt lgkmcnt(0)\n"
    "v_add_u32 v249, 13056, v248\n"
    "v_add_u32 v250, 15232, v248\n"
    "ds_read2_b64 v[96:99], v249 offset0:0 offset1:2\n"
    "ds_read2_b64 v[100:103], v249 offset0:136 offset1:138\n"
    "ds_read2_b64 v[104:107], v250 offset0:0 offset1:2\n"
    "ds_read2_b64 v[108:111], v250 offset0:136 offset1:138\n"
    "v_cvt_pk_bf16_f32 v128, v128, v129\n"
    "v_cvt_pk_bf16_f32 v129, v130, v131\n"
    "v_cvt_pk_bf16_f32 v130, v132, v133\n"
    "v_cvt_pk_bf16_f32 v131, v134, v135\n"
    "ds_write_b128 v247, v[128:131] offset:17408\n"
    "v_cvt_pk_bf16_f32 v136, v136, v137\n"
    "v_cvt_pk_bf16_f32 v137, v138, v139\n"
    "v_cvt_pk_bf16_f32 v138, v140, v141\n"
    "v_cvt_pk_bf16_f32 v139, v142, v143\n"
    "ds_write_b128 v247, v[136:139] offset:17472\n"
    "v_cvt_pk_bf16_f32 v144, v144, v145\n"
    "v_cvt_pk_bf16_f32 v145, v146, v147\n"
    "v_cvt_pk_bf16_f32 v146, v148, v149\n"
    "v_cvt_pk_bf16_f32 v147, v150, v151\n"
    "ds_write_b128 v247, v[144:147] offset:17536\n"
    "v_cvt_pk_bf16_f32 v152, v152, v153\n"
    "v_cvt_pk_bf16_f32 v153, v154, v155\n"
    "v_cvt_pk_bf16_f32 v154, v156, v157\n"
    "v_cvt_pk_bf16_f32 v155, v158, v159\n"
    "ds_write_b128 v247, v[152:155] offset:17600\n"
    "s_mov_b32 s52, 0x4e800000\n"
    "v_cmp_nge_f32 s[54:55], s52, v240\n"
    "s_nop 3\n"
    "s_cmp_lg_u64 s[54:55], 0\n"
    "s_cselect_b32 %0, 1, 0\n"
    "s_waitcnt lgkmcnt(0)\n"
    "v_add_u32 v249, 17408, v248\n"
    "v_add_u32 v250, 19584, v248\n"
    "ds_read2_b64 v[128:131], v249 offset0:0 offset1:2\n"
    "ds_read2_b64 v[132:135], v249 offset0:136 offset1:138\n"
    "ds_read2_b64 v[136:139], v250 offset0:0 offset1:2\n"
    "ds_read2_b64 v[140:143], v250 offset0:136 offset1:138\n"
    "s_waitcnt lgkmcnt(4)\n"
    "s_add_u32 s43, s42, 12288\n"
    "buffer_store_dwordx4 v[96:99], v229, s[96:99], s43 offen sc0 sc1\n"
    "buffer_store_dwordx4 v[100:103], v229, s[96:99], s43 offen offset:1024 sc0 sc1\n"
    "buffer_store_dwordx4 v[104:107], v229, s[96:99], s43 offen offset:2048 sc0 sc1\n"
    "buffer_store_dwordx4 v[108:111], v229, s[96:99], s43 offen offset:3072 sc0 sc1\n"
    "s_waitcnt lgkmcnt(0)\n"
    "s_add_u32 s43, s42, 16384\n"
    "buffer_store_dwordx4 v[128:131], v229, s[96:99], s43 offen sc0 sc1\n"
    "buffer_store_dwordx4 v[132:135], v229, s[96:99], s43 offen offset:1024 sc0 sc1\n"
    "buffer_store_dwordx4 v[136:139], v229, s[96:99], s43 offen offset:2048 sc0 sc1\n"
    "buffer_store_dwordx4 v[140:143], v229, s[96:99], s43 offen offset:3072 sc0 sc1\n"
    "s_branch END%=\n"
    "OI1%=:\n"
    "s_add_u32 s52, s94, 16384\n"
    "s_and_b32 s52, s52, 131071\n"
    "s_add_u32 m0, s40, 114688\n"
    "s_add_u32 s43, s41, s52\n"
    "buffer_load_dwordx4 v231, s[36:39], s43 offen nt lds\n"
    "buffer_load_dwordx4 v232, s[36:39], s43 offen offset:1024 nt lds\n"
    "buffer_load_dwordx4 v233, s[36:39], s43 offen offset:2048 nt lds\n"
    "buffer_load_dwordx4 v234, s[36:39], s43 offen offset:3072 nt lds\n"
    "s_branch JI1%=\n"
    "OI2%=:\n"
    "s_add_u32 s52, s94, 24576\n"
    "s_and_b32 s52, s52, 131071\n"
    "s_add_u32 m0, s58, 0\n"
    "s_nop 0\n"
    "buffer_load_dwordx4 v225, s[20:23], s52 offen nt lds\n"
    "buffer_load_dwordx4 v226, s[20:23], s52 offen offset:1024 nt lds\n"
    "buffer_load_dwordx4 v227, s[20:23], s52 offen offset:2048 nt lds\n"
    "buffer_load_dwordx4 v228, s[20:23], s52 offen offset:3072 nt lds\n"
    "s_branch JI2%=\n"
    "OI3%=:\n"
    "s_add_u32 s52, s94, 24576\n"
    "s_and_b32 s52, s52, 131071\n"
    "s_add_u32 s52, s52, 4096\n"
    "s_add_u32 m0, s58, 4096\n"
    "s_nop 0\n"
    "buffer_load_dwordx4 v225, s[20:23], s52 offen nt lds\n"
    "buffer_load_dwordx4 v226, s[20:23], s52 offen offset:1024 nt lds\n"
    "buffer_load_dwordx4 v227, s[20:23], s52 offen offset:2048 nt lds\n"
    "buffer_load_dwordx4 v228, s[20:23], s52 offen offset:3072 nt lds\n"
    "s_add_u32 s52, s94, 24576\n"
    "s_and_b32 s52, s52, 131071\n"
    "s_add_u32 m0, s58, 8192\n"
    "s_nop 0\n"
    "buffer_load_dwordx4 v229, s[24:27], s52 offen nt lds\n"
    "buffer_load_dwordx4 v230, s[24:27], s52 offen offset:1024 nt lds\n"
    "buffer_load_dwordx4 v229, s[24:27], s52 offen offset:2048 nt lds\n"
    "buffer_load_dwordx4 v230, s[24:27], s52 offen offset:3072 nt lds\n"
    "s_add_u32 s52, s94, 24576\n"
    "s_and_b32 s52, s52, 131071\n"
    "s_add_u32 s52, s52, 4096\n"
    "s_add_u32 m0, s58, 12288\n"
    "s_nop 0\n"
    "buffer_load_dwordx4 v229, s[24:27], s52 offen nt lds\n"
    "buffer_load_dwordx4 v230, s[24:27], s52 offen offset:1024 nt lds\n"
    "buffer_load_dwordx4 v229, s[24:27], s52 offen offset:2048 nt lds\n"
    "buffer_load_dwordx4 v230, s[24:27], s52 offen offset:3072 nt lds\n"
    "s_branch JI3%=\n"
    "OWQ4%=:\n"
    "s_waitcnt vmcnt(20)\n"
    "s_branch JWQ4%=\n"
    "OI4%=:\n"
    "s_add_u32 s52, s94, 32768\n"
    "s_and_b32 s52, s52, 131071\n"
    "s_add_u32 m0, s58, 16384\n"
    "s_nop 0\n"
    "buffer_load_dwordx4 v225, s[20:23], s52 offen nt lds\n"
    "buffer_load_dwordx4 v226, s[20:23], s52 offen offset:1024 nt lds\n"
    "buffer_load_dwordx4 v227, s[20:23], s52 offen offset:2048 nt lds\n"
    "buffer_load_dwordx4 v228, s[20:23], s52 offen offset:3072 nt lds\n"
    "s_add_u32 s52, s94, 32768\n"
    "s_and_b32 s52, s52, 131071\n"
    "s_add_u32 s52, s52, 4096\n"
    "s_add_u32 m0, s58, 20480\n"
    "s_nop 0\n"
    "buffer_load_dwordx4 v225, s[20:23], s52 offen nt lds\n"
    "buffer_load_dwordx4 v226, s[20:23], s52 offen offset:1024 nt lds\n"
    "buffer_load_dwordx4 v227, s[20:23], s52 offen offset:2048 nt lds\n"
    "buffer_load_dwordx4 v228, s[20:23], s52 offen offset:3072 nt lds\n"
    "s_add_u32 s52, s94, 32768\n"
    "s_and_b32 s52, s52, 131071\n"
    "s_add_u32 m0, s58, 24576\n"
    "s_nop 0\n"
    "buffer_load_dwordx4 v229, s[24:27], s52 offen nt lds\n"
    "buffer_load_dwordx4 v230, s[24:27], s52 offen offset:1024 nt lds\n"
    "buffer_load_dwordx4 v229, s[24:27], s52 offen offset:2048 nt lds\n"
    "buffer_load_dwordx4 v230, s[24:27], s52 offen offset:3072 nt lds\n"
    "s_add_u32 s52, s94, 32768\n"
    "s_and_b32 s52, s52, 131071\n"
    "s_add_u32 s52, s52, 4096\n"
    "s_add_u32 m0, s58, 28672\n"
    "s_nop 0\n"
    "buffer_load_dwordx4 v229, s[24:27], s52 offen nt lds\n"
    "buffer_load_dwordx4 v230, s[24:27], s52 offen offset:1024 nt lds\n"
    "buffer_load_dwordx4 v229, s[24:27], s52 offen offset:2048 nt lds\n"
    "buffer_load_dwordx4 v230, s[24:27], s52 offen offset:3072 nt lds\n"
    "s_branch JI4%=\n"
    "OS5%=:\n"
    "v_mfma_f32_16x16x32_bf16 v[128:131], a[192:195], v[212:215], v[128:131]\n"
    "v_mfma_f32_16x16x32_bf16 v[132:135], a[196:199], v[212:215], v[132:135]\n"
    "v_mfma_f32_16x16x32_bf16 v[136:139], a[200:203], v[212:215], v[136:139]\n"
    "v_mfma_f32_16x16x32_bf16 v[140:143], a[204:207], v[212:215], v[140:143]\n"
    "v_mfma_f32_16x16x32_bf16 v[144:147], a[208:211], v[212:215], v[144:147]\n"
    "v_mfma_f32_16x16x32_bf16 v[148:151], a[212:215], v[212:215], v[148:151]\n"
    "v_mfma_f32_16x16x32_bf16 v[152:155], a[216:219], v[212:215], v[152:155]\n"
    "v_mfma_f32_16x16x32_bf16 v[156:159], a[220:223], v[212:215], v[156:159]\n"
    "s_branch JS5%=\n"
    "OT2%=:\n"
    "s_waitcnt vmcnt(32)\n"
    "s_branch JT2%=\n"
    "OF5%=:\n"
    "s_add_u32 s52, s94, 40960\n"
    "s_and_b32 s52, s52, 131071\n"
    "s_add_u32 m0, s58, 32768\n"
    "s_nop 0\n"
    "buffer_load_dwordx4 v225, s[20:23], s52 offen nt lds\n"
    "buffer_load_dwordx4 v226, s[20:23], s52 offen offset:1024 nt lds\n"
    "buffer_load_dwordx4 v227, s[20:23], s52 offen offset:2048 nt lds\n"
    "buffer_load_dwordx4 v228, s[20:23], s52 offen offset:3072 nt lds\n"
    "s_add_u32 s52, s94, 40960\n"
    "s_and_b32 s52, s52, 131071\n"
    "s_add_u32 s52, s52, 4096\n"
    "s_add_u32 m0, s58, 36864\n"
    "s_nop 0\n"
    "buffer_load_dwordx4 v225, s[20:23], s52 offen nt lds\n"
    "buffer_load_dwordx4 v226, s[20:23], s52 offen offset:1024 nt lds\n"
    "buffer_load_dwordx4 v227, s[20:23], s52 offen offset:2048 nt lds\n"
    "buffer_load_dwordx4 v228, s[20:23], s52 offen offset:3072 nt lds\n"
    "s_add_u32 s52, s94, 40960\n"
    "s_and_b32 s52, s52, 131071\n"
    "s_add_u32 m0, s58, 40960\n"
    "s_nop 0\n"
    "buffer_load_dwordx4 v229, s[24:27], s52 offen nt lds\n"
    "buffer_load_dwordx4 v230, s[24:27], s52 offen offset:1024 nt lds\n"
    "buffer_load_dwordx4 v229, s[24:27], s52 offen offset:2048 nt lds\n"
    "buffer_load_dwordx4 v230, s[24:27], s52 offen offset:3072 nt lds\n"
    "s_add_u32 s52, s94, 40960\n"
    "s_and_b32 s52, s52, 131071\n"
    "s_add_u32 s52, s52, 4096\n"
    "s_add_u32 m0, s58, 45056\n"
    "s_nop 0\n"
    "buffer_load_dwordx4 v229, s[24:27], s52 offen nt lds\n"
    "buffer_load_dwordx4 v230, s[24:27], s52 offen offset:1024 nt lds\n"
    "buffer_load_dwordx4 v229, s[24:27], s52 offen offset:2048 nt lds\n"
    "buffer_load_dwordx4 v230, s[24:27], s52 offen offset:3072 nt lds\n"
    "s_branch JF5%=\n"
    "OT3%=:\n"
    "s_waitcnt vmcnt(32)\n"
    "s_branch JT3%=\n"
    "WBB%=:\n"
    "s_cmp_eq_u32 s44, 12\n"
    "s_cbranch_scc0 WBB2%=\n"
    "s_mov_b32 s22, 0\n"
    "s_mov_b32 s26, 0\n"
    "s_mov_b32 s47, 0\n"
    "s_mov_b32 s45, 0\n"
    "s_branch WRB%=\n"
    "WBB2%=:\n"
    "s_branch WRB%=\n"
    "END%=:\n"
    ".Lattn_main_end:\n"
    : "=&s"(unsafe)
    : "s"(qh), "s"(kh), "s"(vh), "s"(oh), "s"(w), "s"(rot)
    : "memory", "vcc", "scc", "s20","s21","s22","s23","s24","s25","s26","s27","s28","s29","s30","s31","s36","s37","s38","s39","s40","s41","s42","s43","s44","s45","s46","s47","s48","s49","s50","s51","s52","s53","s54","s55","s56","s57","s58","s59","s60","s61","s62","s63","s64","s65","s66","s67","s68","s69","s70","s71","s72","s73","s74","s75","s76","s77","s78","s79","s80","s81","s82","s83","s84","s85","s86","s87","s88","s89","s90","s91","s92","s93","s94","s95","s96","s97","s98","s99",
      "v0","v1","v2","v3","v4","v5","v6","v7","v8","v9","v10","v11","v12","v13","v14","v15","v16","v17","v18","v19","v20","v21","v22","v23","v24","v25","v26","v27","v28","v29","v30","v31","v32","v33","v34","v35","v36","v37","v38","v39","v40","v41","v42","v43","v44","v45","v46","v47","v48","v49","v50","v51","v52","v53","v54","v55","v56","v57","v58","v59","v60","v61","v62","v63","v64","v65","v66","v67","v68","v69","v70","v71","v72","v73","v74","v75","v76","v77","v78","v79","v80","v81","v82","v83","v84","v85","v86","v87","v88","v89","v90","v91","v92","v93","v94","v95","v96","v97","v98","v99","v100","v101","v102","v103","v104","v105","v106","v107","v108","v109","v110","v111","v112","v113","v114","v115","v116","v117","v118","v119","v120","v121","v122","v123","v124","v125","v126","v127","v128","v129","v130","v131","v132","v133","v134","v135","v136","v137","v138","v139","v140","v141","v142","v143","v144","v145","v146","v147","v148","v149","v150","v151","v152","v153","v154","v155","v156","v157","v158","v159","v160","v161","v162","v163","v164","v165","v166","v167","v168","v169","v170","v171","v172","v173","v174","v175","v176","v177","v178","v179","v180","v181","v182","v183","v184","v185","v186","v187","v188","v189","v190","v191","v192","v193","v194","v195","v196","v197","v198","v199","v200","v201","v202","v203","v204","v205","v206","v207","v208","v209","v210","v211","v212","v213","v214","v215","v216","v217","v218","v219","v220","v221","v222","v223","v224","v225","v226","v227","v228","v229","v230","v231","v232","v233","v234","v235","v236","v237","v238","v239","v240","v241","v242","v243","v244","v245","v246","v247","v248","v249","v250","v251","v252","v253","v254","v255",
      "a0","a1","a2","a3","a4","a5","a6","a7","a8","a9","a10","a11","a12","a13","a14","a15","a16","a17","a18","a19","a20","a21","a22","a23","a24","a25","a26","a27","a28","a29","a30","a31","a32","a33","a34","a35","a36","a37","a38","a39","a40","a41","a42","a43","a44","a45","a46","a47","a48","a49","a50","a51","a52","a53","a54","a55","a56","a57","a58","a59","a60","a61","a62","a63","a64","a65","a66","a67","a68","a69","a70","a71","a72","a73","a74","a75","a76","a77","a78","a79","a80","a81","a82","a83","a84","a85","a86","a87","a88","a89","a90","a91","a92","a93","a94","a95","a96","a97","a98","a99","a100","a101","a102","a103","a104","a105","a106","a107","a108","a109","a110","a111","a112","a113","a114","a115","a116","a117","a118","a119","a120","a121","a122","a123","a124","a125","a126","a127","a128","a129","a130","a131","a132","a133","a134","a135","a136","a137","a138","a139","a140","a141","a142","a143","a144","a145","a146","a147","a148","a149","a150","a151","a152","a153","a154","a155","a156","a157","a158","a159","a160","a161","a162","a163","a164","a165","a166","a167","a168","a169","a170","a171","a172","a173","a174","a175","a176","a177","a178","a179","a180","a181","a182","a183","a184","a185","a186","a187","a188","a189","a190","a191","a192","a193","a194","a195","a196","a197","a198","a199","a200","a201","a202","a203","a204","a205","a206","a207","a208","a209","a210","a211","a212","a213","a214","a215","a216","a217","a218","a219","a220","a221","a222","a223","a224","a225","a226","a227","a228","a229","a230","a231","a232","a233","a234","a235","a236","a237","a238","a239","a240","a241","a242","a243","a244","a245","a246","a247","a248","a249","a250","a251","a252","a253","a254","a255");
  if (unsafe) {
    asm volatile("s_waitcnt vmcnt(0)" ::: "memory");
    safe_rows_impl(qh, kh, vh, oh, 96 * w, w == 3 ? 5 : 6);
  }
}

extern "C" void kernel_launch(void* const* d_in, const int* in_sizes, int n_in, void* d_out, int out_size, void* d_ws, size_t ws_size, hipStream_t stream) {
  (void)in_sizes; (void)n_in; (void)out_size; (void)ws_size;
  (void)hipFuncSetAttribute((const void*)attn_fwd_kernel, hipFuncAttributeMaxDynamicSharedMemorySize, LDS_BYTES);
  hipLaunchKernelGGL(attn_fwd_kernel, dim3(NBH), dim3(256), LDS_BYTES, stream, (const u16*)d_in[0], (const u16*)d_in[1], (const u16*)d_in[2], (u16*)d_out);
}
```
